```python
import torch

def _softmax(x, axis=-1):
    return torch.softmax(x, dim=axis)
def _gelu(x):
    return torch.nn.functional.gelu(x, approximate="tanh")
def _silu(x):
    return torch.nn.functional.silu(x)
def _sigmoid(x):
    return torch.sigmoid(x)
def _layernorm(x, axis=-1, eps=1e-5):
    m = x.mean(dim=axis, keepdim=True)
    v = x.var(dim=axis, keepdim=True, unbiased=False)
    return (x - m) / torch.sqrt(v + eps)
def _scatter_add(tgt, idx, src, axis=0):
    return tgt.clone().index_add_(axis, idx, src)
def _segment_sum(src, seg, n):
    out = torch.zeros((n,) + src.shape[1:], dtype=src.dtype)
    return out.index_add_(0, seg, src)
def _pad(x, pw):
    flat = tuple(v for pair in reversed(pw) for v in pair)
    return torch.nn.functional.pad(x, flat)

kernel_name = 'standalone__self_sup_conv_stem_hubert__speech'

def setup_inputs():

    _w = (lambda a: (lambda t: t.to(torch.bfloat16) if t.is_floating_point() else t)(torch.as_tensor(a)))
    B, T, D, K = 2, 4000, 256, 15
    scale = (1.0 / (K * D)) ** 0.5
    return {
        "x": _w((lambda *s: torch.randn(*s, dtype=torch.bfloat16) * 0.05)(B, T, D)),
        "w": _w((lambda *s: torch.randn(*s, dtype=torch.bfloat16) * 0.05)(K, D, D) * scale),
        "b": _w((lambda *s: torch.randn(*s, dtype=torch.bfloat16) * 0.05)(D) * 0.1),
        "gamma": _w(torch.ones((D,), dtype=float)),
        "beta": _w(torch.zeros((D,), dtype=float)),
    }

def reference(x, w, b, gamma, beta):
    B, T, D = x.shape
    K = w.shape[0]
    s = 2
    L = (T - K) // s + 1
    y = torch.einsum('blc,cd->bld', x[:, 0 : s * L : s, :], w[0])
    for i in range(1, K):
        y = y + torch.einsum('blc,cd->bld', x[:, i : i + s * L : s, :], w[i])
    y = y + b[None, None, :]
    mu = y.mean(1, keepdims=True)
    var = ((y - mu) ** 2).mean(1, keepdims=True)
    yn = (y - mu) / torch.sqrt(var + 1e-5)
    yn = yn * gamma[None, None, :] + beta[None, None, :]
    return _gelu(yn)
```

```cpp
#include <hip/hip_runtime.h>
#include <hip/hip_bf16.h>
typedef __hip_bfloat16 bf16;
constexpr int B=2,T=4000,D=256,KT=15,L=1993;

__global__ void conv_naive(const bf16* x, const bf16* w, const bf16* b, float* y) {
    int l = blockIdx.x; int bb = blockIdx.y; int d = threadIdx.x;
    __shared__ float xs[KT*D];
    for (int i = d; i < KT*D; i += 256) xs[i] = __bfloat162float(x[((size_t)bb*T + 2*l)*D + i]);
    __syncthreads();
    float acc = 0.f;
    for (int k = 0; k < KT*D; ++k) acc += xs[k] * __bfloat162float(w[(size_t)k*D + d]);
    y[((size_t)bb*L + l)*D + d] = acc + __bfloat162float(b[d]);
}
__global__ void norm_naive(const float* y, const bf16* gamma, const bf16* beta, bf16* out) {
    int bb = blockIdx.y; int d = blockIdx.x*64 + (threadIdx.x & 63); int r = threadIdx.x >> 6;
    __shared__ float red[4][64];
    float s = 0.f;
    for (int l = r; l < L; l += 4) s += y[((size_t)bb*L + l)*D + d];
    red[r][threadIdx.x&63] = s; __syncthreads();
    float mu = (red[0][threadIdx.x&63]+red[1][threadIdx.x&63]+red[2][threadIdx.x&63]+red[3][threadIdx.x&63]) / L;
    __syncthreads();
    float v = 0.f;
    for (int l = r; l < L; l += 4) { float t = y[((size_t)bb*L + l)*D + d] - mu; v += t*t; }
    red[r][threadIdx.x&63] = v; __syncthreads();
    float var = (red[0][threadIdx.x&63]+red[1][threadIdx.x&63]+red[2][threadIdx.x&63]+red[3][threadIdx.x&63]) / L;
    float rs = rsqrtf(var + 1e-5f);
    float g = __bfloat162float(gamma[d]), be = __bfloat162float(beta[d]);
    for (int l = r; l < L; l += 4) {
        float t = (y[((size_t)bb*L + l)*D + d] - mu) * rs * g + be;
        float u = 0.7978845608028654f * (t + 0.044715f * t*t*t);
        float o = 0.5f * t * (1.f + tanhf(u));
        out[((size_t)bb*L + l)*D + d] = __float2bfloat16(o);
    }
}
extern "C" void kernel_launch(void* const* d_in, const int* in_sizes, int n_in,
                              void* d_out, int out_size, void* d_ws, size_t ws_size,
                              hipStream_t stream) {
    const bf16* x = (const bf16*)d_in[0]; const bf16* w = (const bf16*)d_in[1]; const bf16* b = (const bf16*)d_in[2];
    const bf16* gamma = (const bf16*)d_in[3]; const bf16* beta = (const bf16*)d_in[4];
    float* y = (float*)d_ws;
    conv_naive<<<dim3(L, B), 256, 0, stream>>>(x, w, b, y);
    norm_naive<<<dim3(4, B), 256, 0, stream>>>(y, gamma, beta, (bf16*)d_out);
}
```

```cpp
#include <hip/hip_runtime.h>
#include <hip/hip_bf16.h>

#define LAS __attribute__((address_space(3)))
#define GAS __attribute__((address_space(1)))
typedef __bf16 bf16x2 __attribute__((ext_vector_type(2)));
typedef float f32x2 __attribute__((ext_vector_type(2)));
typedef float f32x4 __attribute__((ext_vector_type(4)));
typedef unsigned u32x4 __attribute__((ext_vector_type(4)));
typedef unsigned u32x2 __attribute__((ext_vector_type(2)));
typedef unsigned short u16;

constexpr int D_ = 256, L_ = 1993;
constexpr int XREG = 144 * 128;
constexpr int X1OFF = 0, X0OFF = 122880;
constexpr int WOFF = 2 * XREG;
constexpr int PRM_OFF = X0OFF + 2 * XREG;
constexpr int EPO_OFF = PRM_OFF + 768;
constexpr int RES_OFF = PRM_OFF + 1024;
constexpr int LDS_BYTES = PRM_OFF + 2048;
constexpr int YT_OFF = 0, YT_STRIDE = 36;
constexpr int WS_REC = 4096;
constexpr int WS_X = 1 << 20;
constexpr int WS_BYTES = WS_X + 256 * 8192;
constexpr int SPIN_CAP = 1 << 16;
constexpr unsigned CODE_SPAN = 13696;

#define MAIN_LOOP_ASM \
    "v_mov_b64 v[24:25], 0\n\t" \
    "v_mov_b64 v[26:27], 0\n\t" \
    "v_mov_b64 v[28:29], 0\n\t" \
    "v_mov_b64 v[30:31], 0\n\t" \
    "v_mov_b64 v[32:33], 0\n\t" \
    "v_mov_b64 v[34:35], 0\n\t" \
    "v_mov_b64 v[36:37], 0\n\t" \
    "v_mov_b64 v[38:39], 0\n\t" \
    "v_mov_b64 v[40:41], 0\n\t" \
    "v_mov_b64 v[42:43], 0\n\t" \
    "v_mov_b64 v[44:45], 0\n\t" \
    "v_mov_b64 v[46:47], 0\n\t" \
    "v_mov_b64 v[48:49], 0\n\t" \
    "v_mov_b64 v[50:51], 0\n\t" \
    "v_mov_b64 v[52:53], 0\n\t" \
    "v_mov_b64 v[54:55], 0\n\t" \
    "s_barrier\n\t" \
    "ds_read_b64_tr_b16 v[140:141], %[wb0] offset:0\n\t" \
    "ds_read_b64_tr_b16 v[142:143], %[wb0] offset:512\n\t" \
    "ds_read_b64_tr_b16 v[144:145], %[wb1] offset:0\n\t" \
    "ds_read_b64_tr_b16 v[146:147], %[wb1] offset:512\n\t" \
    "ds_read_b128 v[56:59], %[xb0] offset:0\n\t" \
    "ds_read_b128 v[60:63], %[xb0] offset:128\n\t" \
    "ds_read_b128 v[64:67], %[xb0] offset:256\n\t" \
    "ds_read_b128 v[68:71], %[xb0] offset:384\n\t" \
    "ds_read_b64_tr_b16 v[148:149], %[wb0] offset:4096\n\t" \
    "ds_read_b64_tr_b16 v[150:151], %[wb0] offset:4608\n\t" \
    "ds_read_b64_tr_b16 v[152:153], %[wb1] offset:4096\n\t" \
    "ds_read_b64_tr_b16 v[154:155], %[wb1] offset:4608\n\t" \
    "ds_read_b128 v[72:75], %[xb1] offset:0\n\t" \
    "L_loop_%=:\n\t" \
    "s_waitcnt lgkmcnt(5)\n\t" \
    "v_mfma_f32_16x16x32_bf16 v[24:27], v[56:59], v[140:143], v[24:27]\n\t" \
    "s_barrier\n\t" \
    "v_mfma_f32_16x16x32_bf16 v[40:43], v[56:59], v[144:147], v[40:43]\n\t" \
    "ds_read_b64_tr_b16 v[156:157], %[wb0] offset:8192\n\t" \
    "v_mfma_f32_16x16x32_bf16 v[28:31], v[60:63], v[140:143], v[28:31]\n\t" \
    "ds_read_b64_tr_b16 v[158:159], %[wb0] offset:8704\n\t" \
    "v_mfma_f32_16x16x32_bf16 v[44:47], v[60:63], v[144:147], v[44:47]\n\t" \
    "ds_read_b64_tr_b16 v[160:161], %[wb1] offset:8192\n\t" \
    "v_mfma_f32_16x16x32_bf16 v[32:35], v[64:67], v[140:143], v[32:35]\n\t" \
    "ds_read_b64_tr_b16 v[162:163], %[wb1] offset:8704\n\t" \
    "v_mfma_f32_16x16x32_bf16 v[48:51], v[64:67], v[144:147], v[48:51]\n\t" \
    "ds_read_b128 v[76:79], %[xb1] offset:128\n\t" \
    "v_mfma_f32_16x16x32_bf16 v[36:39], v[68:71], v[140:143], v[36:39]\n\t" \
    "s_xor_b32 %[dl], %[dl], 0xfffe2000\n\t" \
    "v_mfma_f32_16x16x32_bf16 v[52:55], v[68:71], v[144:147], v[52:55]\n\t" \
    "s_waitcnt lgkmcnt(5)\n\t" \
    "v_mfma_f32_16x16x32_bf16 v[24:27], v[60:63], v[148:151], v[24:27]\n\t" \
    "s_barrier\n\t" \
    "v_mfma_f32_16x16x32_bf16 v[40:43], v[60:63], v[152:155], v[40:43]\n\t" \
    "ds_read_b64_tr_b16 v[140:141], %[wb0] offset:12288\n\t" \
    "v_mfma_f32_16x16x32_bf16 v[28:31], v[64:67], v[148:151], v[28:31]\n\t" \
    "ds_read_b64_tr_b16 v[142:143], %[wb0] offset:12800\n\t" \
    "v_mfma_f32_16x16x32_bf16 v[44:47], v[64:67], v[152:155], v[44:47]\n\t" \
    "ds_read_b64_tr_b16 v[144:145], %[wb1] offset:12288\n\t" \
    "v_mfma_f32_16x16x32_bf16 v[32:35], v[68:71], v[148:151], v[32:35]\n\t" \
    "ds_read_b64_tr_b16 v[146:147], %[wb1] offset:12800\n\t" \
    "v_mfma_f32_16x16x32_bf16 v[48:51], v[68:71], v[152:155], v[48:51]\n\t" \
    "ds_read_b128 v[80:83], %[xb1] offset:256\n\t" \
    "v_mfma_f32_16x16x32_bf16 v[36:39], v[72:75], v[148:151], v[36:39]\n\t" \
    "v_xad_u32 %[nb0], %[xb0], 64, %[dl]\n\t" \
    "v_mfma_f32_16x16x32_bf16 v[52:55], v[72:75], v[152:155], v[52:55]\n\t" \
    "v_xad_u32 %[nb1], %[xb1], 64, %[dl]\n\t" \
    "s_waitcnt lgkmcnt(5)\n\t" \
    "v_mfma_f32_16x16x32_bf16 v[24:27], v[64:67], v[156:159], v[24:27]\n\t" \
    "s_barrier\n\t" \
    "v_mfma_f32_16x16x32_bf16 v[40:43], v[64:67], v[160:163], v[40:43]\n\t" \
    "ds_read_b64_tr_b16 v[148:149], %[wb0] offset:16384\n\t" \
    "v_mfma_f32_16x16x32_bf16 v[28:31], v[68:71], v[156:159], v[28:31]\n\t" \
    "ds_read_b64_tr_b16 v[150:151], %[wb0] offset:16896\n\t" \
    "v_mfma_f32_16x16x32_bf16 v[44:47], v[68:71], v[160:163], v[44:47]\n\t" \
    "ds_read_b64_tr_b16 v[152:153], %[wb1] offset:16384\n\t" \
    "v_mfma_f32_16x16x32_bf16 v[32:35], v[72:75], v[156:159], v[32:35]\n\t" \
    "ds_read_b64_tr_b16 v[154:155], %[wb1] offset:16896\n\t" \
    "v_mfma_f32_16x16x32_bf16 v[48:51], v[72:75], v[160:163], v[48:51]\n\t" \
    "ds_read_b128 v[84:87], %[xb1] offset:384\n\t" \
    "v_mfma_f32_16x16x32_bf16 v[36:39], v[76:79], v[156:159], v[36:39]\n\t" \
    "v_mfma_f32_16x16x32_bf16 v[52:55], v[76:79], v[160:163], v[52:55]\n\t" \
    "s_waitcnt lgkmcnt(5)\n\t" \
    "v_mfma_f32_16x16x32_bf16 v[24:27], v[68:71], v[140:143], v[24:27]\n\t" \
    "s_barrier\n\t" \
    "v_mfma_f32_16x16x32_bf16 v[40:43], v[68:71], v[144:147], v[40:43]\n\t" \
    "ds_read_b64_tr_b16 v[156:157], %[wb0] offset:20480\n\t" \
    "v_mfma_f32_16x16x32_bf16 v[28:31], v[72:75], v[140:143], v[28:31]\n\t" \
    "ds_read_b64_tr_b16 v[158:159], %[wb0] offset:20992\n\t" \
    "v_mfma_f32_16x16x32_bf16 v[44:47], v[72:75], v[144:147], v[44:47]\n\t" \
    "ds_read_b64_tr_b16 v[160:161], %[wb1] offset:20480\n\t" \
    "v_mfma_f32_16x16x32_bf16 v[32:35], v[76:79], v[140:143], v[32:35]\n\t" \
    "ds_read_b64_tr_b16 v[162:163], %[wb1] offset:20992\n\t" \
    "v_mfma_f32_16x16x32_bf16 v[48:51], v[76:79], v[144:147], v[48:51]\n\t" \
    "ds_read_b128 v[88:91], %[xb2] offset:0\n\t" \
    "v_mfma_f32_16x16x32_bf16 v[36:39], v[80:83], v[140:143], v[36:39]\n\t" \
    "ds_read_b128 v[100:103], %[xb0] offset:18432\n\t" \
    "v_mfma_f32_16x16x32_bf16 v[52:55], v[80:83], v[144:147], v[52:55]\n\t" \
    "s_waitcnt lgkmcnt(6)\n\t" \
    "v_mfma_f32_16x16x32_bf16 v[24:27], v[72:75], v[148:151], v[24:27]\n\t" \
    "s_barrier\n\t" \
    "v_mfma_f32_16x16x32_bf16 v[40:43], v[72:75], v[152:155], v[40:43]\n\t" \
    "ds_read_b64_tr_b16 v[140:141], %[wb0] offset:24576\n\t" \
    "v_mfma_f32_16x16x32_bf16 v[28:31], v[76:79], v[148:151], v[28:31]\n\t" \
    "ds_read_b64_tr_b16 v[142:143], %[wb0] offset:25088\n\t" \
    "v_mfma_f32_16x16x32_bf16 v[44:47], v[76:79], v[152:155], v[44:47]\n\t" \
    "ds_read_b64_tr_b16 v[144:145], %[wb1] offset:24576\n\t" \
    "v_mfma_f32_16x16x32_bf16 v[32:35], v[80:83], v[148:151], v[32:35]\n\t" \
    "ds_read_b64_tr_b16 v[146:147], %[wb1] offset:25088\n\t" \
    "v_mfma_f32_16x16x32_bf16 v[48:51], v[80:83], v[152:155], v[48:51]\n\t" \
    "ds_read_b128 v[92:95], %[xb2] offset:128\n\t" \
    "v_mfma_f32_16x16x32_bf16 v[36:39], v[84:87], v[148:151], v[36:39]\n\t" \
    "ds_read_b128 v[104:107], %[xb0] offset:18560\n\t" \
    "v_mfma_f32_16x16x32_bf16 v[52:55], v[84:87], v[152:155], v[52:55]\n\t" \
    "s_waitcnt lgkmcnt(6)\n\t" \
    "v_mfma_f32_16x16x32_bf16 v[24:27], v[76:79], v[156:159], v[24:27]\n\t" \
    "s_barrier\n\t" \
    "v_mfma_f32_16x16x32_bf16 v[40:43], v[76:79], v[160:163], v[40:43]\n\t" \
    "ds_read_b64_tr_b16 v[148:149], %[wb0] offset:28672\n\t" \
    "v_mfma_f32_16x16x32_bf16 v[28:31], v[80:83], v[156:159], v[28:31]\n\t" \
    "ds_read_b64_tr_b16 v[150:151], %[wb0] offset:29184\n\t" \
    "v_mfma_f32_16x16x32_bf16 v[44:47], v[80:83], v[160:163], v[44:47]\n\t" \
    "ds_read_b64_tr_b16 v[152:153], %[wb1] offset:28672\n\t" \
    "v_mfma_f32_16x16x32_bf16 v[32:35], v[84:87], v[156:159], v[32:35]\n\t" \
    "ds_read_b64_tr_b16 v[154:155], %[wb1] offset:29184\n\t" \
    "v_mfma_f32_16x16x32_bf16 v[48:51], v[84:87], v[160:163], v[48:51]\n\t" \
    "ds_read_b128 v[96:99], %[xb2] offset:256\n\t" \
    "v_mfma_f32_16x16x32_bf16 v[36:39], v[88:91], v[156:159], v[36:39]\n\t" \
    "ds_read_b128 v[108:111], %[xb0] offset:18688\n\t" \
    "v_mfma_f32_16x16x32_bf16 v[52:55], v[88:91], v[160:163], v[52:55]\n\t" \
    "s_waitcnt lgkmcnt(6)\n\t" \
    "v_mfma_f32_16x16x32_bf16 v[24:27], v[80:83], v[140:143], v[24:27]\n\t" \
    "s_barrier\n\t" \
    "v_mfma_f32_16x16x32_bf16 v[40:43], v[80:83], v[144:147], v[40:43]\n\t" \
    "ds_read_b64_tr_b16 v[156:157], %[wb0] offset:32768\n\t" \
    "v_mfma_f32_16x16x32_bf16 v[28:31], v[84:87], v[140:143], v[28:31]\n\t" \
    "ds_read_b64_tr_b16 v[158:159], %[wb0] offset:33280\n\t" \
    "v_mfma_f32_16x16x32_bf16 v[44:47], v[84:87], v[144:147], v[44:47]\n\t" \
    "ds_read_b64_tr_b16 v[160:161], %[wb1] offset:32768\n\t" \
    "v_mfma_f32_16x16x32_bf16 v[32:35], v[88:91], v[140:143], v[32:35]\n\t" \
    "ds_read_b64_tr_b16 v[162:163], %[wb1] offset:33280\n\t" \
    "v_mfma_f32_16x16x32_bf16 v[48:51], v[88:91], v[144:147], v[48:51]\n\t" \
    "ds_read_b128 v[112:115], %[xb0] offset:18816\n\t" \
    "v_mfma_f32_16x16x32_bf16 v[36:39], v[92:95], v[140:143], v[36:39]\n\t" \
    "v_mfma_f32_16x16x32_bf16 v[52:55], v[92:95], v[144:147], v[52:55]\n\t" \
    "s_waitcnt lgkmcnt(5)\n\t" \
    "v_mfma_f32_16x16x32_bf16 v[24:27], v[84:87], v[148:151], v[24:27]\n\t" \
    "s_barrier\n\t" \
    "v_mfma_f32_16x16x32_bf16 v[40:43], v[84:87], v[152:155], v[40:43]\n\t" \
    "ds_read_b64_tr_b16 v[140:141], %[wb0] offset:36864\n\t" \
    "v_mfma_f32_16x16x32_bf16 v[28:31], v[88:91], v[148:151], v[28:31]\n\t" \
    "ds_read_b64_tr_b16 v[142:143], %[wb0] offset:37376\n\t" \
    "v_mfma_f32_16x16x32_bf16 v[44:47], v[88:91], v[152:155], v[44:47]\n\t" \
    "ds_read_b64_tr_b16 v[144:145], %[wb1] offset:36864\n\t" \
    "v_mfma_f32_16x16x32_bf16 v[32:35], v[92:95], v[148:151], v[32:35]\n\t" \
    "ds_read_b64_tr_b16 v[146:147], %[wb1] offset:37376\n\t" \
    "v_mfma_f32_16x16x32_bf16 v[48:51], v[92:95], v[152:155], v[48:51]\n\t" \
    "ds_read_b128 v[116:119], %[xb1] offset:18432\n\t" \
    "v_mfma_f32_16x16x32_bf16 v[36:39], v[96:99], v[148:151], v[36:39]\n\t" \
    "v_mfma_f32_16x16x32_bf16 v[52:55], v[96:99], v[152:155], v[52:55]\n\t" \
    "s_waitcnt lgkmcnt(5)\n\t" \
    "v_mfma_f32_16x16x32_bf16 v[24:27], v[100:103], v[156:159], v[24:27]\n\t" \
    "s_barrier\n\t" \
    "v_mfma_f32_16x16x32_bf16 v[40:43], v[100:103], v[160:163], v[40:43]\n\t" \
    "ds_read_b64_tr_b16 v[148:149], %[wb0] offset:40960\n\t" \
    "v_mfma_f32_16x16x32_bf16 v[28:31], v[104:107], v[156:159], v[28:31]\n\t" \
    "ds_read_b64_tr_b16 v[150:151], %[wb0] offset:41472\n\t" \
    "v_mfma_f32_16x16x32_bf16 v[44:47], v[104:107], v[160:163], v[44:47]\n\t" \
    "ds_read_b64_tr_b16 v[152:153], %[wb1] offset:40960\n\t" \
    "v_mfma_f32_16x16x32_bf16 v[32:35], v[108:111], v[156:159], v[32:35]\n\t" \
    "ds_read_b64_tr_b16 v[154:155], %[wb1] offset:41472\n\t" \
    "v_mfma_f32_16x16x32_bf16 v[48:51], v[108:111], v[160:163], v[48:51]\n\t" \
    "ds_read_b128 v[120:123], %[xb1] offset:18560\n\t" \
    "v_mfma_f32_16x16x32_bf16 v[36:39], v[112:115], v[156:159], v[36:39]\n\t" \
    "v_mfma_f32_16x16x32_bf16 v[52:55], v[112:115], v[160:163], v[52:55]\n\t" \
    "s_waitcnt lgkmcnt(5)\n\t" \
    "v_mfma_f32_16x16x32_bf16 v[24:27], v[104:107], v[140:143], v[24:27]\n\t" \
    "s_barrier\n\t" \
    "v_mfma_f32_16x16x32_bf16 v[40:43], v[104:107], v[144:147], v[40:43]\n\t" \
    "ds_read_b64_tr_b16 v[156:157], %[wb0] offset:45056\n\t" \
    "v_mfma_f32_16x16x32_bf16 v[28:31], v[108:111], v[140:143], v[28:31]\n\t" \
    "ds_read_b64_tr_b16 v[158:159], %[wb0] offset:45568\n\t" \
    "v_mfma_f32_16x16x32_bf16 v[44:47], v[108:111], v[144:147], v[44:47]\n\t" \
    "ds_read_b64_tr_b16 v[160:161], %[wb1] offset:45056\n\t" \
    "v_mfma_f32_16x16x32_bf16 v[32:35], v[112:115], v[140:143], v[32:35]\n\t" \
    "ds_read_b64_tr_b16 v[162:163], %[wb1] offset:45568\n\t" \
    "v_mfma_f32_16x16x32_bf16 v[48:51], v[112:115], v[144:147], v[48:51]\n\t" \
    "ds_read_b128 v[124:127], %[xb1] offset:18688\n\t" \
    "v_mfma_f32_16x16x32_bf16 v[36:39], v[116:119], v[140:143], v[36:39]\n\t" \
    "s_sub_u32 %[cnt], %[cnt], 1\n\t" \
    "v_mfma_f32_16x16x32_bf16 v[52:55], v[116:119], v[144:147], v[52:55]\n\t" \
    "s_waitcnt lgkmcnt(5)\n\t" \
    "v_mfma_f32_16x16x32_bf16 v[24:27], v[108:111], v[148:151], v[24:27]\n\t" \
    "s_barrier\n\t" \
    "v_mfma_f32_16x16x32_bf16 v[40:43], v[108:111], v[152:155], v[40:43]\n\t" \
    "ds_read_b64_tr_b16 v[140:141], %[wb0] offset:49152\n\t" \
    "v_mfma_f32_16x16x32_bf16 v[28:31], v[112:115], v[148:151], v[28:31]\n\t" \
    "ds_read_b64_tr_b16 v[142:143], %[wb0] offset:49664\n\t" \
    "v_mfma_f32_16x16x32_bf16 v[44:47], v[112:115], v[152:155], v[44:47]\n\t" \
    "ds_read_b64_tr_b16 v[144:145], %[wb1] offset:49152\n\t" \
    "v_mfma_f32_16x16x32_bf16 v[32:35], v[116:119], v[148:151], v[32:35]\n\t" \
    "ds_read_b64_tr_b16 v[146:147], %[wb1] offset:49664\n\t" \
    "v_mfma_f32_16x16x32_bf16 v[48:51], v[116:119], v[152:155], v[48:51]\n\t" \
    "ds_read_b128 v[128:131], %[xb1] offset:18816\n\t" \
    "v_mfma_f32_16x16x32_bf16 v[36:39], v[120:123], v[148:151], v[36:39]\n\t" \
    "ds_read_b128 v[56:59], %[nb0] offset:0\n\t" \
    "v_mfma_f32_16x16x32_bf16 v[52:55], v[120:123], v[152:155], v[52:55]\n\t" \
    "s_waitcnt lgkmcnt(6)\n\t" \
    "v_mfma_f32_16x16x32_bf16 v[24:27], v[112:115], v[156:159], v[24:27]\n\t" \
    "s_barrier\n\t" \
    "v_mfma_f32_16x16x32_bf16 v[40:43], v[112:115], v[160:163], v[40:43]\n\t" \
    "ds_read_b64_tr_b16 v[148:149], %[wb0] offset:53248\n\t" \
    "v_mfma_f32_16x16x32_bf16 v[28:31], v[116:119], v[156:159], v[28:31]\n\t" \
    "ds_read_b64_tr_b16 v[150:151], %[wb0] offset:53760\n\t" \
    "v_mfma_f32_16x16x32_bf16 v[44:47], v[116:119], v[160:163], v[44:47]\n\t" \
    "ds_read_b64_tr_b16 v[152:153], %[wb1] offset:53248\n\t" \
    "v_mfma_f32_16x16x32_bf16 v[32:35], v[120:123], v[156:159], v[32:35]\n\t" \
    "ds_read_b64_tr_b16 v[154:155], %[wb1] offset:53760\n\t" \
    "v_mfma_f32_16x16x32_bf16 v[48:51], v[120:123], v[160:163], v[48:51]\n\t" \
    "ds_read_b128 v[132:135], %[xb2] offset:18432\n\t" \
    "v_mfma_f32_16x16x32_bf16 v[36:39], v[124:127], v[156:159], v[36:39]\n\t" \
    "ds_read_b128 v[60:63], %[nb0] offset:128\n\t" \
    "v_mfma_f32_16x16x32_bf16 v[52:55], v[124:127], v[160:163], v[52:55]\n\t" \
    "s_waitcnt lgkmcnt(6)\n\t" \
    "v_mfma_f32_16x16x32_bf16 v[24:27], v[116:119], v[140:143], v[24:27]\n\t" \
    "s_barrier\n\t" \
    "v_mfma_f32_16x16x32_bf16 v[40:43], v[116:119], v[144:147], v[40:43]\n\t" \
    "ds_read_b64_tr_b16 v[156:157], %[wb0] offset:57344\n\t" \
    "v_mfma_f32_16x16x32_bf16 v[28:31], v[120:123], v[140:143], v[28:31]\n\t" \
    "ds_read_b64_tr_b16 v[158:159], %[wb0] offset:57856\n\t" \
    "v_mfma_f32_16x16x32_bf16 v[44:47], v[120:123], v[144:147], v[44:47]\n\t" \
    "ds_read_b64_tr_b16 v[160:161], %[wb1] offset:57344\n\t" \
    "v_mfma_f32_16x16x32_bf16 v[32:35], v[124:127], v[140:143], v[32:35]\n\t" \
    "ds_read_b64_tr_b16 v[162:163], %[wb1] offset:57856\n\t" \
    "v_mfma_f32_16x16x32_bf16 v[48:51], v[124:127], v[144:147], v[48:51]\n\t" \
    "ds_read_b128 v[136:139], %[xb2] offset:18560\n\t" \
    "v_mfma_f32_16x16x32_bf16 v[36:39], v[128:131], v[140:143], v[36:39]\n\t" \
    "ds_read_b128 v[64:67], %[nb0] offset:256\n\t" \
    "v_mfma_f32_16x16x32_bf16 v[52:55], v[128:131], v[144:147], v[52:55]\n\t" \
    "s_waitcnt lgkmcnt(6)\n\t" \
    "v_mfma_f32_16x16x32_bf16 v[24:27], v[120:123], v[148:151], v[24:27]\n\t" \
    "s_barrier\n\t" \
    "v_mfma_f32_16x16x32_bf16 v[40:43], v[120:123], v[152:155], v[40:43]\n\t" \
    "ds_read_b64_tr_b16 v[140:141], %[wb0] offset:0\n\t" \
    "v_mfma_f32_16x16x32_bf16 v[28:31], v[124:127], v[148:151], v[28:31]\n\t" \
    "ds_read_b64_tr_b16 v[142:143], %[wb0] offset:512\n\t" \
    "v_mfma_f32_16x16x32_bf16 v[44:47], v[124:127], v[152:155], v[44:47]\n\t" \
    "ds_read_b64_tr_b16 v[144:145], %[wb1] offset:0\n\t" \
    "v_mfma_f32_16x16x32_bf16 v[32:35], v[128:131], v[148:151], v[32:35]\n\t" \
    "ds_read_b64_tr_b16 v[146:147], %[wb1] offset:512\n\t" \
    "v_mfma_f32_16x16x32_bf16 v[48:51], v[128:131], v[152:155], v[48:51]\n\t" \
    "ds_read_b128 v[68:71], %[nb0] offset:384\n\t" \
    "v_mfma_f32_16x16x32_bf16 v[36:39], v[132:135], v[148:151], v[36:39]\n\t" \
    "v_mov_b32 %[xb0], %[nb0]\n\t" \
    "v_mfma_f32_16x16x32_bf16 v[52:55], v[132:135], v[152:155], v[52:55]\n\t" \
    "v_xad_u32 %[xb2], %[xb2], 64, %[dl]\n\t" \
    "s_waitcnt lgkmcnt(5)\n\t" \
    "v_mfma_f32_16x16x32_bf16 v[24:27], v[124:127], v[156:159], v[24:27]\n\t" \
    "s_barrier\n\t" \
    "v_mfma_f32_16x16x32_bf16 v[40:43], v[124:127], v[160:163], v[40:43]\n\t" \
    "ds_read_b64_tr_b16 v[148:149], %[wb0] offset:4096\n\t" \
    "v_mfma_f32_16x16x32_bf16 v[28:31], v[128:131], v[156:159], v[28:31]\n\t" \
    "ds_read_b64_tr_b16 v[150:151], %[wb0] offset:4608\n\t" \
    "v_mfma_f32_16x16x32_bf16 v[44:47], v[128:131], v[160:163], v[44:47]\n\t" \
    "ds_read_b64_tr_b16 v[152:153], %[wb1] offset:4096\n\t" \
    "v_mfma_f32_16x16x32_bf16 v[32:35], v[132:135], v[156:159], v[32:35]\n\t" \
    "ds_read_b64_tr_b16 v[154:155], %[wb1] offset:4608\n\t" \
    "v_mfma_f32_16x16x32_bf16 v[48:51], v[132:135], v[160:163], v[48:51]\n\t" \
    "ds_read_b128 v[72:75], %[nb1] offset:0\n\t" \
    "v_mfma_f32_16x16x32_bf16 v[36:39], v[136:139], v[156:159], v[36:39]\n\t" \
    "v_mov_b32 %[xb1], %[nb1]\n\t" \
    "v_mfma_f32_16x16x32_bf16 v[52:55], v[136:139], v[160:163], v[52:55]\n\t" \
    "s_waitcnt vmcnt(0)\n\t" \
    "s_cbranch_scc0 L_loop_%=\n\t" \
    "s_waitcnt lgkmcnt(5)\n\t" \
    "v_mfma_f32_16x16x32_bf16 v[40:43], v[56:59], v[144:147], v[40:43]\n\t" \
    "s_barrier\n\t" \
    "ds_read_b64_tr_b16 v[156:157], %[wb1] offset:8192\n\t" \
    "v_mfma_f32_16x16x32_bf16 v[44:47], v[60:63], v[144:147], v[44:47]\n\t" \
    "ds_read_b64_tr_b16 v[158:159], %[wb1] offset:8704\n\t" \
    "ds_read_b64_tr_b16 v[160:161], %[wb1] offset:12288\n\t" \
    "v_mfma_f32_16x16x32_bf16 v[48:51], v[64:67], v[144:147], v[48:51]\n\t" \
    "ds_read_b64_tr_b16 v[162:163], %[wb1] offset:12800\n\t" \
    "ds_read_b128 v[76:79], %[xb1] offset:128\n\t" \
    "v_mfma_f32_16x16x32_bf16 v[52:55], v[68:71], v[144:147], v[52:55]\n\t" \
    "ds_read_b128 v[80:83], %[xb1] offset:256\n\t" \
    "s_waitcnt lgkmcnt(6)\n\t" \
    "v_mfma_f32_16x16x32_bf16 v[40:43], v[60:63], v[152:155], v[40:43]\n\t" \
    "ds_read_b64_tr_b16 v[140:141], %[wb1] offset:16384\n\t" \
    "ds_read_b64_tr_b16 v[142:143], %[wb1] offset:16896\n\t" \
    "v_mfma_f32_16x16x32_bf16 v[44:47], v[64:67], v[152:155], v[44:47]\n\t" \
    "ds_read_b64_tr_b16 v[148:149], %[wb1] offset:20480\n\t" \
    "ds_read_b64_tr_b16 v[150:151], %[wb1] offset:20992\n\t" \
    "v_mfma_f32_16x16x32_bf16 v[48:51], v[68:71], v[152:155], v[48:51]\n\t" \
    "ds_read_b128 v[84:87], %[xb1] offset:384\n\t" \
    "ds_read_b128 v[88:91], %[xb2] offset:0\n\t" \
    "v_mfma_f32_16x16x32_bf16 v[52:55], v[72:75], v[152:155], v[52:55]\n\t" \
    "s_waitcnt lgkmcnt(7)\n\t" \
    "v_mfma_f32_16x16x32_bf16 v[40:43], v[64:67], v[156:159], v[40:43]\n\t" \
    "s_barrier\n\t" \
    "ds_read_b64_tr_b16 v[144:145], %[wb1] offset:24576\n\t" \
    "v_mfma_f32_16x16x32_bf16 v[44:47], v[68:71], v[156:159], v[44:47]\n\t" \
    "ds_read_b64_tr_b16 v[146:147], %[wb1] offset:25088\n\t" \
    "ds_read_b128 v[92:95], %[xb2] offset:128\n\t" \
    "v_mfma_f32_16x16x32_bf16 v[48:51], v[72:75], v[156:159], v[48:51]\n\t" \
    "v_add_u32 %[nb0], 0xf000, %[wb0]\n\t" \
    "v_add_u32 %[nb1], 0xf000, %[wb1]\n\t" \
    "v_mfma_f32_16x16x32_bf16 v[52:55], v[76:79], v[156:159], v[52:55]\n\t" \
    "s_waitcnt lgkmcnt(9)\n\t" \
    "v_mfma_f32_16x16x32_bf16 v[40:43], v[68:71], v[160:163], v[40:43]\n\t" \
    "ds_read_b64_tr_b16 v[152:153], %[wb1] offset:28672\n\t" \
    "v_mfma_f32_16x16x32_bf16 v[44:47], v[72:75], v[160:163], v[44:47]\n\t" \
    "ds_read_b64_tr_b16 v[154:155], %[wb1] offset:29184\n\t" \
    "v_mfma_f32_16x16x32_bf16 v[48:51], v[76:79], v[160:163], v[48:51]\n\t" \
    "ds_read_b128 v[96:99], %[xb2] offset:256\n\t" \
    "v_mfma_f32_16x16x32_bf16 v[52:55], v[80:83], v[160:163], v[52:55]\n\t" \
    "ds_read_b128 v[100:103], %[xb0] offset:18432\n\t" \
    "s_waitcnt lgkmcnt(8)\n\t" \
    "v_mfma_f32_16x16x32_bf16 v[40:43], v[72:75], v[140:143], v[40:43]\n\t" \
    "s_barrier\n\t" \
    "ds_read_b64_tr_b16 v[156:157], %[wb1] offset:32768\n\t" \
    "v_mfma_f32_16x16x32_bf16 v[44:47], v[76:79], v[140:143], v[44:47]\n\t" \
    "ds_read_b64_tr_b16 v[158:159], %[wb1] offset:33280\n\t" \
    "ds_read_b128 v[104:107], %[xb0] offset:18560\n\t" \
    "v_mfma_f32_16x16x32_bf16 v[48:51], v[80:83], v[140:143], v[48:51]\n\t" \
    "ds_read_b128 v[108:111], %[xb0] offset:18688\n\t" \
    "v_mfma_f32_16x16x32_bf16 v[52:55], v[84:87], v[140:143], v[52:55]\n\t" \
    "s_waitcnt lgkmcnt(9)\n\t" \
    "v_mfma_f32_16x16x32_bf16 v[40:43], v[76:79], v[148:151], v[40:43]\n\t" \
    "ds_read_b64_tr_b16 v[160:161], %[wb1] offset:36864\n\t" \
    "v_mfma_f32_16x16x32_bf16 v[44:47], v[80:83], v[148:151], v[44:47]\n\t" \
    "ds_read_b64_tr_b16 v[162:163], %[wb1] offset:37376\n\t" \
    "v_mfma_f32_16x16x32_bf16 v[48:51], v[84:87], v[148:151], v[48:51]\n\t" \
    "ds_read_b128 v[112:115], %[xb0] offset:18816\n\t" \
    "v_mfma_f32_16x16x32_bf16 v[52:55], v[88:91], v[148:151], v[52:55]\n\t" \
    "ds_read_b128 v[116:119], %[xb1] offset:18432\n\t" \
    "s_waitcnt lgkmcnt(9)\n\t" \
    "v_mfma_f32_16x16x32_bf16 v[40:43], v[80:83], v[144:147], v[40:43]\n\t" \
    "s_barrier\n\t" \
    "ds_read_b64_tr_b16 v[140:141], %[wb1] offset:40960\n\t" \
    "v_mfma_f32_16x16x32_bf16 v[44:47], v[84:87], v[144:147], v[44:47]\n\t" \
    "ds_read_b64_tr_b16 v[142:143], %[wb1] offset:41472\n\t" \
    "ds_read_b128 v[120:123], %[xb1] offset:18560\n\t" \
    "v_mfma_f32_16x16x32_bf16 v[48:51], v[88:91], v[144:147], v[48:51]\n\t" \
    "ds_read_b128 v[124:127], %[xb1] offset:18688\n\t" \
    "v_mfma_f32_16x16x32_bf16 v[52:55], v[92:95], v[144:147], v[52:55]\n\t" \
    "s_waitcnt lgkmcnt(9)\n\t" \
    "v_mfma_f32_16x16x32_bf16 v[40:43], v[84:87], v[152:155], v[40:43]\n\t" \
    "ds_read_b64_tr_b16 v[148:149], %[wb1] offset:45056\n\t" \
    "v_mfma_f32_16x16x32_bf16 v[44:47], v[88:91], v[152:155], v[44:47]\n\t" \
    "ds_read_b64_tr_b16 v[150:151], %[wb1] offset:45568\n\t" \
    "v_mfma_f32_16x16x32_bf16 v[48:51], v[92:95], v[152:155], v[48:51]\n\t" \
    "ds_read_b128 v[128:131], %[xb1] offset:18816\n\t" \
    "v_mfma_f32_16x16x32_bf16 v[52:55], v[96:99], v[152:155], v[52:55]\n\t" \
    "ds_read_b128 v[132:135], %[xb2] offset:18432\n\t" \
    "s_waitcnt lgkmcnt(9)\n\t" \
    "v_mfma_f32_16x16x32_bf16 v[40:43], v[100:103], v[156:159], v[40:43]\n\t" \
    "s_barrier\n\t" \
    "v_mfma_f32_16x16x32_bf16 v[44:47], v[104:107], v[156:159], v[44:47]\n\t" \
    "ds_read_b64_tr_b16 v[144:145], %[wb1] offset:49152\n\t" \
    "v_mfma_f32_16x16x32_bf16 v[48:51], v[108:111], v[156:159], v[48:51]\n\t" \
    "ds_read_b64_tr_b16 v[146:147], %[wb1] offset:49664\n\t" \
    "v_mfma_f32_16x16x32_bf16 v[52:55], v[112:115], v[156:159], v[52:55]\n\t" \
    "ds_read_b128 v[136:139], %[xb2] offset:18560\n\t" \
    "s_waitcnt lgkmcnt(9)\n\t" \
    "v_mfma_f32_16x16x32_bf16 v[40:43], v[104:107], v[160:163], v[40:43]\n\t" \
    "ds_read_b64_tr_b16 v[152:153], %[wb1] offset:53248\n\t" \
    "ds_read_b64_tr_b16 v[154:155], %[wb1] offset:53760\n\t" \
    "v_mfma_f32_16x16x32_bf16 v[44:47], v[108:111], v[160:163], v[44:47]\n\t" \
    "v_xor_b32 %[xb0], 64, %[xb0]\n\t" \
    "v_xor_b32 %[xb1], 64, %[xb1]\n\t" \
    "v_mfma_f32_16x16x32_bf16 v[48:51], v[112:115], v[160:163], v[48:51]\n\t" \
    "v_xor_b32 %[xb2], 64, %[xb2]\n\t" \
    "v_mfma_f32_16x16x32_bf16 v[52:55], v[116:119], v[160:163], v[52:55]\n\t" \
    "s_waitcnt lgkmcnt(9)\n\t" \
    "v_mfma_f32_16x16x32_bf16 v[40:43], v[108:111], v[140:143], v[40:43]\n\t" \
    "s_barrier\n\t" \
    "ds_read_b64_tr_b16 v[156:157], %[wb1] offset:57344\n\t" \
    "v_mfma_f32_16x16x32_bf16 v[44:47], v[112:115], v[140:143], v[44:47]\n\t" \
    "ds_read_b64_tr_b16 v[158:159], %[wb1] offset:57856\n\t" \
    "ds_read_b128 v[56:59], %[xb0] offset:0\n\t" \
    "v_mfma_f32_16x16x32_bf16 v[48:51], v[116:119], v[140:143], v[48:51]\n\t" \
    "ds_read_b128 v[60:63], %[xb0] offset:128\n\t" \
    "v_mfma_f32_16x16x32_bf16 v[52:55], v[120:123], v[140:143], v[52:55]\n\t" \
    "s_waitcnt lgkmcnt(9)\n\t" \
    "v_mfma_f32_16x16x32_bf16 v[40:43], v[112:115], v[148:151], v[40:43]\n\t" \
    "ds_read_b64_tr_b16 v[160:161], %[nb1] offset:0\n\t" \
    "v_mfma_f32_16x16x32_bf16 v[44:47], v[116:119], v[148:151], v[44:47]\n\t" \
    "ds_read_b64_tr_b16 v[162:163], %[nb1] offset:512\n\t" \
    "v_mfma_f32_16x16x32_bf16 v[48:51], v[120:123], v[148:151], v[48:51]\n\t" \
    "ds_read_b128 v[64:67], %[xb0] offset:256\n\t" \
    "v_mfma_f32_16x16x32_bf16 v[52:55], v[124:127], v[148:151], v[52:55]\n\t" \
    "ds_read_b128 v[68:71], %[xb0] offset:384\n\t" \
    "s_waitcnt lgkmcnt(9)\n\t" \
    "v_mfma_f32_16x16x32_bf16 v[40:43], v[116:119], v[144:147], v[40:43]\n\t" \
    "s_barrier\n\t" \
    "ds_read_b64_tr_b16 v[140:141], %[nb1] offset:4096\n\t" \
    "v_mfma_f32_16x16x32_bf16 v[44:47], v[120:123], v[144:147], v[44:47]\n\t" \
    "ds_read_b64_tr_b16 v[142:143], %[nb1] offset:4608\n\t" \
    "ds_read_b128 v[72:75], %[xb1] offset:0\n\t" \
    "v_mfma_f32_16x16x32_bf16 v[48:51], v[124:127], v[144:147], v[48:51]\n\t" \
    "ds_read_b128 v[76:79], %[xb1] offset:128\n\t" \
    "v_mfma_f32_16x16x32_bf16 v[52:55], v[128:131], v[144:147], v[52:55]\n\t" \
    "s_waitcnt lgkmcnt(9)\n\t" \
    "v_mfma_f32_16x16x32_bf16 v[40:43], v[120:123], v[152:155], v[40:43]\n\t" \
    "ds_read_b64_tr_b16 v[148:149], %[nb1] offset:8192\n\t" \
    "v_mfma_f32_16x16x32_bf16 v[44:47], v[124:127], v[152:155], v[44:47]\n\t" \
    "ds_read_b64_tr_b16 v[150:151], %[nb1] offset:8704\n\t" \
    "v_mfma_f32_16x16x32_bf16 v[48:51], v[128:131], v[152:155], v[48:51]\n\t" \
    "ds_read_b128 v[80:83], %[xb1] offset:256\n\t" \
    "v_mfma_f32_16x16x32_bf16 v[52:55], v[132:135], v[152:155], v[52:55]\n\t" \
    "ds_read_b128 v[84:87], %[xb1] offset:384\n\t" \
    "s_waitcnt lgkmcnt(9)\n\t" \
    "v_mfma_f32_16x16x32_bf16 v[40:43], v[124:127], v[156:159], v[40:43]\n\t" \
    "s_barrier\n\t" \
    "ds_read_b64_tr_b16 v[144:145], %[nb1] offset:12288\n\t" \
    "v_mfma_f32_16x16x32_bf16 v[44:47], v[128:131], v[156:159], v[44:47]\n\t" \
    "ds_read_b64_tr_b16 v[146:147], %[nb1] offset:12800\n\t" \
    "ds_read_b128 v[88:91], %[xb2] offset:0\n\t" \
    "v_mfma_f32_16x16x32_bf16 v[48:51], v[132:135], v[156:159], v[48:51]\n\t" \
    "ds_read_b128 v[92:95], %[xb2] offset:128\n\t" \
    "v_mfma_f32_16x16x32_bf16 v[52:55], v[136:139], v[156:159], v[52:55]\n\t" \
    "s_waitcnt lgkmcnt(9)\n\t" \
    "v_mfma_f32_16x16x32_bf16 v[40:43], v[56:59], v[160:163], v[40:43]\n\t" \
    "ds_read_b64_tr_b16 v[152:153], %[nb1] offset:16384\n\t" \
    "v_mfma_f32_16x16x32_bf16 v[44:47], v[60:63], v[160:163], v[44:47]\n\t" \
    "ds_read_b64_tr_b16 v[154:155], %[nb1] offset:16896\n\t" \
    "v_mfma_f32_16x16x32_bf16 v[48:51], v[64:67], v[160:163], v[48:51]\n\t" \
    "ds_read_b128 v[96:99], %[xb2] offset:256\n\t" \
    "v_mfma_f32_16x16x32_bf16 v[52:55], v[68:71], v[160:163], v[52:55]\n\t" \
    "ds_read_b128 v[100:103], %[xb0] offset:18432\n\t" \
    "s_waitcnt lgkmcnt(9)\n\t" \
    "v_mfma_f32_16x16x32_bf16 v[40:43], v[60:63], v[140:143], v[40:43]\n\t" \
    "s_barrier\n\t" \
    "ds_read_b64_tr_b16 v[156:157], %[nb1] offset:20480\n\t" \
    "v_mfma_f32_16x16x32_bf16 v[44:47], v[64:67], v[140:143], v[44:47]\n\t" \
    "ds_read_b64_tr_b16 v[158:159], %[nb1] offset:20992\n\t" \
    "ds_read_b128 v[104:107], %[xb0] offset:18560\n\t" \
    "v_mfma_f32_16x16x32_bf16 v[48:51], v[68:71], v[140:143], v[48:51]\n\t" \
    "ds_read_b128 v[108:111], %[xb0] offset:18688\n\t" \
    "v_mfma_f32_16x16x32_bf16 v[52:55], v[72:75], v[140:143], v[52:55]\n\t" \
    "s_waitcnt lgkmcnt(9)\n\t" \
    "v_mfma_f32_16x16x32_bf16 v[40:43], v[64:67], v[148:151], v[40:43]\n\t" \
    "ds_read_b64_tr_b16 v[160:161], %[nb1] offset:24576\n\t" \
    "v_mfma_f32_16x16x32_bf16 v[44:47], v[68:71], v[148:151], v[44:47]\n\t" \
    "ds_read_b64_tr_b16 v[162:163], %[nb1] offset:25088\n\t" \
    "v_mfma_f32_16x16x32_bf16 v[48:51], v[72:75], v[148:151], v[48:51]\n\t" \
    "ds_read_b128 v[112:115], %[xb0] offset:18816\n\t" \
    "v_mfma_f32_16x16x32_bf16 v[52:55], v[76:79], v[148:151], v[52:55]\n\t" \
    "ds_read_b128 v[116:119], %[xb1] offset:18432\n\t" \
    "s_waitcnt lgkmcnt(9)\n\t" \
    "v_mfma_f32_16x16x32_bf16 v[40:43], v[68:71], v[144:147], v[40:43]\n\t" \
    "s_barrier\n\t" \
    "ds_read_b64_tr_b16 v[140:141], %[nb1] offset:28672\n\t" \
    "v_mfma_f32_16x16x32_bf16 v[44:47], v[72:75], v[144:147], v[44:47]\n\t" \
    "ds_read_b64_tr_b16 v[142:143], %[nb1] offset:29184\n\t" \
    "ds_read_b128 v[120:123], %[xb1] offset:18560\n\t" \
    "v_mfma_f32_16x16x32_bf16 v[48:51], v[76:79], v[144:147], v[48:51]\n\t" \
    "ds_read_b128 v[124:127], %[xb1] offset:18688\n\t" \
    "v_mfma_f32_16x16x32_bf16 v[52:55], v[80:83], v[144:147], v[52:55]\n\t" \
    "s_waitcnt lgkmcnt(9)\n\t" \
    "v_mfma_f32_16x16x32_bf16 v[40:43], v[72:75], v[152:155], v[40:43]\n\t" \
    "ds_read_b64_tr_b16 v[148:149], %[nb1] offset:32768\n\t" \
    "v_mfma_f32_16x16x32_bf16 v[44:47], v[76:79], v[152:155], v[44:47]\n\t" \
    "ds_read_b64_tr_b16 v[150:151], %[nb1] offset:33280\n\t" \
    "v_mfma_f32_16x16x32_bf16 v[48:51], v[80:83], v[152:155], v[48:51]\n\t" \
    "ds_read_b128 v[128:131], %[xb1] offset:18816\n\t" \
    "v_mfma_f32_16x16x32_bf16 v[52:55], v[84:87], v[152:155], v[52:55]\n\t" \
    "ds_read_b128 v[132:135], %[xb2] offset:18432\n\t" \
    "s_waitcnt lgkmcnt(9)\n\t" \
    "v_mfma_f32_16x16x32_bf16 v[40:43], v[76:79], v[156:159], v[40:43]\n\t" \
    "s_barrier\n\t" \
    "v_mfma_f32_16x16x32_bf16 v[44:47], v[80:83], v[156:159], v[44:47]\n\t" \
    "ds_read_b64_tr_b16 v[144:145], %[nb1] offset:36864\n\t" \
    "v_mfma_f32_16x16x32_bf16 v[48:51], v[84:87], v[156:159], v[48:51]\n\t" \
    "ds_read_b64_tr_b16 v[146:147], %[nb1] offset:37376\n\t" \
    "v_mfma_f32_16x16x32_bf16 v[52:55], v[88:91], v[156:159], v[52:55]\n\t" \
    "ds_read_b128 v[136:139], %[xb2] offset:18560\n\t" \
    "s_waitcnt lgkmcnt(9)\n\t" \
    "v_mfma_f32_16x16x32_bf16 v[40:43], v[80:83], v[160:163], v[40:43]\n\t" \
    "ds_read_b64_tr_b16 v[152:153], %[nb1] offset:40960\n\t" \
    "v_mfma_f32_16x16x32_bf16 v[44:47], v[84:87], v[160:163], v[44:47]\n\t" \
    "ds_read_b64_tr_b16 v[154:155], %[nb1] offset:41472\n\t" \
    "v_mfma_f32_16x16x32_bf16 v[48:51], v[88:91], v[160:163], v[48:51]\n\t" \
    "v_mfma_f32_16x16x32_bf16 v[52:55], v[92:95], v[160:163], v[52:55]\n\t" \
    "s_waitcnt lgkmcnt(9)\n\t" \
    "v_mfma_f32_16x16x32_bf16 v[40:43], v[84:87], v[140:143], v[40:43]\n\t" \
    "s_barrier\n\t" \
    "v_mfma_f32_16x16x32_bf16 v[44:47], v[88:91], v[140:143], v[44:47]\n\t" \
    "ds_read_b64_tr_b16 v[156:157], %[nb1] offset:45056\n\t" \
    "v_mfma_f32_16x16x32_bf16 v[48:51], v[92:95], v[140:143], v[48:51]\n\t" \
    "ds_read_b64_tr_b16 v[158:159], %[nb1] offset:45568\n\t" \
    "v_mfma_f32_16x16x32_bf16 v[52:55], v[96:99], v[140:143], v[52:55]\n\t" \
    "s_waitcnt lgkmcnt(9)\n\t" \
    "v_mfma_f32_16x16x32_bf16 v[40:43], v[100:103], v[148:151], v[40:43]\n\t" \
    "ds_read_b64_tr_b16 v[160:161], %[nb1] offset:49152\n\t" \
    "v_mfma_f32_16x16x32_bf16 v[44:47], v[104:107], v[148:151], v[44:47]\n\t" \
    "ds_read_b64_tr_b16 v[162:163], %[nb1] offset:49664\n\t" \
    "v_mfma_f32_16x16x32_bf16 v[48:51], v[108:111], v[148:151], v[48:51]\n\t" \
    "v_mfma_f32_16x16x32_bf16 v[52:55], v[112:115], v[148:151], v[52:55]\n\t" \
    "s_waitcnt lgkmcnt(7)\n\t" \
    "v_mfma_f32_16x16x32_bf16 v[40:43], v[104:107], v[144:147], v[40:43]\n\t" \
    "s_barrier\n\t" \
    "v_mfma_f32_16x16x32_bf16 v[44:47], v[108:111], v[144:147], v[44:47]\n\t" \
    "ds_read_b64_tr_b16 v[140:141], %[nb1] offset:53248\n\t" \
    "v_mfma_f32_16x16x32_bf16 v[48:51], v[112:115], v[144:147], v[48:51]\n\t" \
    "ds_read_b64_tr_b16 v[142:143], %[nb1] offset:53760\n\t" \
    "v_mfma_f32_16x16x32_bf16 v[52:55], v[116:119], v[144:147], v[52:55]\n\t" \
    "s_waitcnt lgkmcnt(6)\n\t" \
    "v_mfma_f32_16x16x32_bf16 v[40:43], v[108:111], v[152:155], v[40:43]\n\t" \
    "ds_read_b64_tr_b16 v[148:149], %[nb1] offset:57344\n\t" \
    "v_mfma_f32_16x16x32_bf16 v[44:47], v[112:115], v[152:155], v[44:47]\n\t" \
    "ds_read_b64_tr_b16 v[150:151], %[nb1] offset:57856\n\t" \
    "v_mfma_f32_16x16x32_bf16 v[48:51], v[116:119], v[152:155], v[48:51]\n\t" \
    "v_mfma_f32_16x16x32_bf16 v[52:55], v[120:123], v[152:155], v[52:55]\n\t" \
    "s_waitcnt lgkmcnt(6)\n\t" \
    "v_mfma_f32_16x16x32_bf16 v[40:43], v[112:115], v[156:159], v[40:43]\n\t" \
    "ds_read_b64_tr_b16 v[144:145], %[nb0] offset:0\n\t" \
    "v_mfma_f32_16x16x32_bf16 v[44:47], v[116:119], v[156:159], v[44:47]\n\t" \
    "ds_read_b64_tr_b16 v[146:147], %[nb0] offset:512\n\t" \
    "v_mfma_f32_16x16x32_bf16 v[48:51], v[120:123], v[156:159], v[48:51]\n\t" \
    "v_mfma_f32_16x16x32_bf16 v[52:55], v[124:127], v[156:159], v[52:55]\n\t" \
    "s_waitcnt lgkmcnt(6)\n\t" \
    "v_mfma_f32_16x16x32_bf16 v[40:43], v[116:119], v[160:163], v[40:43]\n\t" \
    "ds_read_b64_tr_b16 v[152:153], %[nb0] offset:4096\n\t" \
    "v_mfma_f32_16x16x32_bf16 v[44:47], v[120:123], v[160:163], v[44:47]\n\t" \
    "ds_read_b64_tr_b16 v[154:155], %[nb0] offset:4608\n\t" \
    "v_mfma_f32_16x16x32_bf16 v[48:51], v[124:127], v[160:163], v[48:51]\n\t" \
    "v_mfma_f32_16x16x32_bf16 v[52:55], v[128:131], v[160:163], v[52:55]\n\t" \
    "s_waitcnt lgkmcnt(6)\n\t" \
    "v_mfma_f32_16x16x32_bf16 v[40:43], v[120:123], v[140:143], v[40:43]\n\t" \
    "ds_read_b64_tr_b16 v[156:157], %[nb0] offset:8192\n\t" \
    "v_mfma_f32_16x16x32_bf16 v[44:47], v[124:127], v[140:143], v[44:47]\n\t" \
    "ds_read_b64_tr_b16 v[158:159], %[nb0] offset:8704\n\t" \
    "v_mfma_f32_16x16x32_bf16 v[48:51], v[128:131], v[140:143], v[48:51]\n\t" \
    "v_mfma_f32_16x16x32_bf16 v[52:55], v[132:135], v[140:143], v[52:55]\n\t" \
    "s_waitcnt lgkmcnt(6)\n\t" \
    "v_mfma_f32_16x16x32_bf16 v[40:43], v[124:127], v[148:151], v[40:43]\n\t" \
    "ds_read_b64_tr_b16 v[160:161], %[nb0] offset:12288\n\t" \
    "v_mfma_f32_16x16x32_bf16 v[44:47], v[128:131], v[148:151], v[44:47]\n\t" \
    "ds_read_b64_tr_b16 v[162:163], %[nb0] offset:12800\n\t" \
    "v_mfma_f32_16x16x32_bf16 v[48:51], v[132:135], v[148:151], v[48:51]\n\t" \
    "v_mfma_f32_16x16x32_bf16 v[52:55], v[136:139], v[148:151], v[52:55]\n\t" \
    "s_waitcnt lgkmcnt(6)\n\t" \
    "v_mfma_f32_16x16x32_bf16 v[24:27], v[56:59], v[144:147], v[24:27]\n\t" \
    "ds_read_b64_tr_b16 v[140:141], %[nb0] offset:16384\n\t" \
    "ds_read_b64_tr_b16 v[142:143], %[nb0] offset:16896\n\t" \
    "v_mfma_f32_16x16x32_bf16 v[28:31], v[60:63], v[144:147], v[28:31]\n\t" \
    "v_cvt_pk_bf16_f32 v8, v40, v41\n\t" \
    "v_cvt_pk_bf16_f32 v9, v42, v43\n\t" \
    "v_mfma_f32_16x16x32_bf16 v[32:35], v[64:67], v[144:147], v[32:35]\n\t" \
    "v_cvt_pk_bf16_f32 v10, v44, v45\n\t" \
    "v_cvt_pk_bf16_f32 v11, v46, v47\n\t" \
    "v_mfma_f32_16x16x32_bf16 v[36:39], v[68:71], v[144:147], v[36:39]\n\t" \
    "v_min_u32 v8, -2, v8\n\t" \
    "v_min_u32 v9, -2, v9\n\t" \
    "s_waitcnt lgkmcnt(6)\n\t" \
    "v_mfma_f32_16x16x32_bf16 v[24:27], v[60:63], v[152:155], v[24:27]\n\t" \
    "v_min_u32 v10, -2, v10\n\t" \
    "v_min_u32 v11, -2, v11\n\t" \
    "v_mfma_f32_16x16x32_bf16 v[28:31], v[64:67], v[152:155], v[28:31]\n\t" \
    "buffer_store_dwordx4 v[8:11], %[l16], %[rs], %[sa] offen offset:0 sc1\n\t" \
    "ds_read_b64_tr_b16 v[148:149], %[nb0] offset:20480\n\t" \
    "v_mfma_f32_16x16x32_bf16 v[32:35], v[68:71], v[152:155], v[32:35]\n\t" \
    "ds_read_b64_tr_b16 v[150:151], %[nb0] offset:20992\n\t" \
    "v_mfma_f32_16x16x32_bf16 v[36:39], v[72:75], v[152:155], v[36:39]\n\t" \
    "s_waitcnt lgkmcnt(6)\n\t" \
    "v_mfma_f32_16x16x32_bf16 v[24:27], v[64:67], v[156:159], v[24:27]\n\t" \
    "ds_read_b64_tr_b16 v[144:145], %[nb0] offset:24576\n\t" \
    "ds_read_b64_tr_b16 v[146:147], %[nb0] offset:25088\n\t" \
    "v_mfma_f32_16x16x32_bf16 v[28:31], v[68:71], v[156:159], v[28:31]\n\t" \
    "v_cvt_pk_bf16_f32 v12, v48, v49\n\t" \
    "v_cvt_pk_bf16_f32 v13, v50, v51\n\t" \
    "v_mfma_f32_16x16x32_bf16 v[32:35], v[72:75], v[156:159], v[32:35]\n\t" \
    "v_cvt_pk_bf16_f32 v14, v52, v53\n\t" \
    "v_cvt_pk_bf16_f32 v15, v54, v55\n\t" \
    "v_mfma_f32_16x16x32_bf16 v[36:39], v[76:79], v[156:159], v[36:39]\n\t" \
    "v_min_u32 v12, -2, v12\n\t" \
    "v_min_u32 v13, -2, v13\n\t" \
    "s_waitcnt lgkmcnt(6)\n\t" \
    "v_mfma_f32_16x16x32_bf16 v[24:27], v[68:71], v[160:163], v[24:27]\n\t" \
    "v_min_u32 v14, -2, v14\n\t" \
    "v_min_u32 v15, -2, v15\n\t" \
    "v_mfma_f32_16x16x32_bf16 v[28:31], v[72:75], v[160:163], v[28:31]\n\t" \
    "buffer_store_dwordx4 v[12:15], %[l16], %[rs], %[sa] offen offset:1024 sc1\n\t" \
    "ds_read_b64_tr_b16 v[152:153], %[nb0] offset:28672\n\t" \
    "v_mfma_f32_16x16x32_bf16 v[32:35], v[76:79], v[160:163], v[32:35]\n\t" \
    "ds_read_b64_tr_b16 v[154:155], %[nb0] offset:29184\n\t" \
    "v_mfma_f32_16x16x32_bf16 v[36:39], v[80:83], v[160:163], v[36:39]\n\t" \
    "s_waitcnt lgkmcnt(6)\n\t" \
    "v_mfma_f32_16x16x32_bf16 v[24:27], v[72:75], v[140:143], v[24:27]\n\t" \
    "ds_read_b64_tr_b16 v[156:157], %[nb0] offset:32768\n\t" \
    "v_mfma_f32_16x16x32_bf16 v[28:31], v[76:79], v[140:143], v[28:31]\n\t" \
    "ds_read_b64_tr_b16 v[158:159], %[nb0] offset:33280\n\t" \
    "v_mfma_f32_16x16x32_bf16 v[32:35], v[80:83], v[140:143], v[32:35]\n\t" \
    "v_mfma_f32_16x16x32_bf16 v[36:39], v[84:87], v[140:143], v[36:39]\n\t" \
    "s_waitcnt lgkmcnt(6)\n\t" \
    "v_mfma_f32_16x16x32_bf16 v[24:27], v[76:79], v[148:151], v[24:27]\n\t" \
    "ds_read_b64_tr_b16 v[160:161], %[nb0] offset:36864\n\t" \
    "v_mfma_f32_16x16x32_bf16 v[28:31], v[80:83], v[148:151], v[28:31]\n\t" \
    "ds_read_b64_tr_b16 v[162:163], %[nb0] offset:37376\n\t" \
    "v_mfma_f32_16x16x32_bf16 v[32:35], v[84:87], v[148:151], v[32:35]\n\t" \
    "v_mfma_f32_16x16x32_bf16 v[36:39], v[88:91], v[148:151], v[36:39]\n\t" \
    "s_waitcnt lgkmcnt(6)\n\t" \
    "v_mfma_f32_16x16x32_bf16 v[24:27], v[80:83], v[144:147], v[24:27]\n\t" \
    "ds_read_b64_tr_b16 v[140:141], %[nb0] offset:40960\n\t" \
    "ds_read_b64_tr_b16 v[142:143], %[nb0] offset:41472\n\t" \
    "v_mfma_f32_16x16x32_bf16 v[28:31], v[84:87], v[144:147], v[28:31]\n\t" \
    "v_xor_b32 %[xb0], 64, %[xb0]\n\t" \
    "v_xor_b32 %[xb1], 64, %[xb1]\n\t" \
    "v_mfma_f32_16x16x32_bf16 v[32:35], v[88:91], v[144:147], v[32:35]\n\t" \
    "v_xor_b32 %[xb2], 64, %[xb2]\n\t" \
    "v_mfma_f32_16x16x32_bf16 v[36:39], v[92:95], v[144:147], v[36:39]\n\t" \
    "s_waitcnt lgkmcnt(6)\n\t" \
    "v_mfma_f32_16x16x32_bf16 v[24:27], v[84:87], v[152:155], v[24:27]\n\t" \
    "ds_read_b64_tr_b16 v[148:149], %[nb0] offset:45056\n\t" \
    "v_mfma_f32_16x16x32_bf16 v[28:31], v[88:91], v[152:155], v[28:31]\n\t" \
    "ds_read_b64_tr_b16 v[150:151], %[nb0] offset:45568\n\t" \
    "v_mfma_f32_16x16x32_bf16 v[32:35], v[92:95], v[152:155], v[32:35]\n\t" \
    "v_mfma_f32_16x16x32_bf16 v[36:39], v[96:99], v[152:155], v[36:39]\n\t" \
    "s_waitcnt lgkmcnt(6)\n\t" \
    "v_mfma_f32_16x16x32_bf16 v[24:27], v[100:103], v[156:159], v[24:27]\n\t" \
    "ds_read_b64_tr_b16 v[144:145], %[nb0] offset:49152\n\t" \
    "v_mfma_f32_16x16x32_bf16 v[28:31], v[104:107], v[156:159], v[28:31]\n\t" \
    "ds_read_b64_tr_b16 v[146:147], %[nb0] offset:49664\n\t" \
    "v_mfma_f32_16x16x32_bf16 v[32:35], v[108:111], v[156:159], v[32:35]\n\t" \
    "ds_read_b128 v[56:59], %[xb0] offset:0\n\t" \
    "v_mfma_f32_16x16x32_bf16 v[36:39], v[112:115], v[156:159], v[36:39]\n\t" \
    "ds_read_b128 v[60:63], %[xb0] offset:128\n\t" \
    "s_waitcnt lgkmcnt(8)\n\t" \
    "v_mfma_f32_16x16x32_bf16 v[24:27], v[104:107], v[160:163], v[24:27]\n\t" \
    "ds_read_b64_tr_b16 v[152:153], %[nb0] offset:53248\n\t" \
    "v_mfma_f32_16x16x32_bf16 v[28:31], v[108:111], v[160:163], v[28:31]\n\t" \
    "ds_read_b64_tr_b16 v[154:155], %[nb0] offset:53760\n\t" \
    "v_mfma_f32_16x16x32_bf16 v[32:35], v[112:115], v[160:163], v[32:35]\n\t" \
    "ds_read_b128 v[64:67], %[xb0] offset:256\n\t" \
    "v_mfma_f32_16x16x32_bf16 v[36:39], v[116:119], v[160:163], v[36:39]\n\t" \
    "ds_read_b128 v[68:71], %[xb0] offset:384\n\t" \
    "s_waitcnt lgkmcnt(9)\n\t" \
    "v_mfma_f32_16x16x32_bf16 v[24:27], v[108:111], v[140:143], v[24:27]\n\t" \
    "ds_read_b64_tr_b16 v[156:157], %[nb0] offset:57344\n\t" \
    "v_mfma_f32_16x16x32_bf16 v[28:31], v[112:115], v[140:143], v[28:31]\n\t" \
    "ds_read_b64_tr_b16 v[158:159], %[nb0] offset:57856\n\t" \
    "v_mfma_f32_16x16x32_bf16 v[32:35], v[116:119], v[140:143], v[32:35]\n\t" \
    "ds_read_b128 v[72:75], %[xb1] offset:0\n\t" \
    "v_mfma_f32_16x16x32_bf16 v[36:39], v[120:123], v[140:143], v[36:39]\n\t" \
    "ds_read_b128 v[76:79], %[xb1] offset:128\n\t" \
    "s_waitcnt lgkmcnt(9)\n\t" \
    "v_mfma_f32_16x16x32_bf16 v[24:27], v[112:115], v[148:151], v[24:27]\n\t" \
    "ds_read_b64_tr_b16 v[160:161], %[wb0] offset:0\n\t" \
    "v_mfma_f32_16x16x32_bf16 v[28:31], v[116:119], v[148:151], v[28:31]\n\t" \
    "ds_read_b64_tr_b16 v[162:163], %[wb0] offset:512\n\t" \
    "v_mfma_f32_16x16x32_bf16 v[32:35], v[120:123], v[148:151], v[32:35]\n\t" \
    "ds_read_b128 v[80:83], %[xb1] offset:256\n\t" \
    "v_mfma_f32_16x16x32_bf16 v[36:39], v[124:127], v[148:151], v[36:39]\n\t" \
    "ds_read_b128 v[84:87], %[xb1] offset:384\n\t" \
    "s_waitcnt lgkmcnt(9)\n\t" \
    "v_mfma_f32_16x16x32_bf16 v[24:27], v[116:119], v[144:147], v[24:27]\n\t" \
    "ds_read_b64_tr_b16 v[140:141], %[wb0] offset:4096\n\t" \
    "v_mfma_f32_16x16x32_bf16 v[28:31], v[120:123], v[144:147], v[28:31]\n\t" \
    "ds_read_b64_tr_b16 v[142:143], %[wb0] offset:4608\n\t" \
    "v_mfma_f32_16x16x32_bf16 v[32:35], v[124:127], v[144:147], v[32:35]\n\t" \
    "ds_read_b128 v[88:91], %[xb2] offset:0\n\t" \
    "v_mfma_f32_16x16x32_bf16 v[36:39], v[128:131], v[144:147], v[36:39]\n\t" \
    "ds_read_b128 v[92:95], %[xb2] offset:128\n\t" \
    "s_waitcnt lgkmcnt(9)\n\t" \
    "v_mfma_f32_16x16x32_bf16 v[24:27], v[120:123], v[152:155], v[24:27]\n\t" \
    "ds_read_b64_tr_b16 v[148:149], %[wb0] offset:8192\n\t" \
    "v_mfma_f32_16x16x32_bf16 v[28:31], v[124:127], v[152:155], v[28:31]\n\t" \
    "ds_read_b64_tr_b16 v[150:151], %[wb0] offset:8704\n\t" \
    "v_mfma_f32_16x16x32_bf16 v[32:35], v[128:131], v[152:155], v[32:35]\n\t" \
    "ds_read_b128 v[96:99], %[xb2] offset:256\n\t" \
    "v_mfma_f32_16x16x32_bf16 v[36:39], v[132:135], v[152:155], v[36:39]\n\t" \
    "s_waitcnt lgkmcnt(9)\n\t" \
    "v_mfma_f32_16x16x32_bf16 v[24:27], v[124:127], v[156:159], v[24:27]\n\t" \
    "ds_read_b64_tr_b16 v[144:145], %[wb0] offset:12288\n\t" \
    "v_mfma_f32_16x16x32_bf16 v[28:31], v[128:131], v[156:159], v[28:31]\n\t" \
    "ds_read_b64_tr_b16 v[146:147], %[wb0] offset:12800\n\t" \
    "v_mfma_f32_16x16x32_bf16 v[32:35], v[132:135], v[156:159], v[32:35]\n\t" \
    "v_mfma_f32_16x16x32_bf16 v[36:39], v[136:139], v[156:159], v[36:39]\n\t" \
    "s_waitcnt lgkmcnt(9)\n\t" \
    "v_mfma_f32_16x16x32_bf16 v[24:27], v[56:59], v[160:163], v[24:27]\n\t" \
    "ds_read_b64_tr_b16 v[152:153], %[wb0] offset:16384\n\t" \
    "v_mfma_f32_16x16x32_bf16 v[28:31], v[60:63], v[160:163], v[28:31]\n\t" \
    "ds_read_b64_tr_b16 v[154:155], %[wb0] offset:16896\n\t" \
    "v_mfma_f32_16x16x32_bf16 v[32:35], v[64:67], v[160:163], v[32:35]\n\t" \
    "ds_read_b128 v[100:103], %[xb0] offset:18432\n\t" \
    "v_mfma_f32_16x16x32_bf16 v[36:39], v[68:71], v[160:163], v[36:39]\n\t" \
    "ds_read_b128 v[104:107], %[xb0] offset:18560\n\t" \
    "s_waitcnt lgkmcnt(9)\n\t" \
    "v_mfma_f32_16x16x32_bf16 v[24:27], v[60:63], v[140:143], v[24:27]\n\t" \
    "s_barrier\n\t" \
    "ds_read_b64_tr_b16 v[156:157], %[wb0] offset:20480\n\t" \
    "v_mfma_f32_16x16x32_bf16 v[28:31], v[64:67], v[140:143], v[28:31]\n\t" \
    "ds_read_b64_tr_b16 v[158:159], %[wb0] offset:20992\n\t" \
    "ds_read_b128 v[108:111], %[xb0] offset:18688\n\t" \
    "v_mfma_f32_16x16x32_bf16 v[32:35], v[68:71], v[140:143], v[32:35]\n\t" \
    "ds_read_b128 v[112:115], %[xb0] offset:18816\n\t" \
    "v_mfma_f32_16x16x32_bf16 v[36:39], v[72:75], v[140:143], v[36:39]\n\t" \
    "s_waitcnt lgkmcnt(9)\n\t" \
    "v_mfma_f32_16x16x32_bf16 v[24:27], v[64:67], v[148:151], v[24:27]\n\t" \
    "ds_read_b64_tr_b16 v[160:161], %[wb0] offset:24576\n\t" \
    "v_mfma_f32_16x16x32_bf16 v[28:31], v[68:71], v[148:151], v[28:31]\n\t" \
    "ds_read_b64_tr_b16 v[162:163], %[wb0] offset:25088\n\t" \
    "v_mfma_f32_16x16x32_bf16 v[32:35], v[72:75], v[148:151], v[32:35]\n\t" \
    "ds_read_b128 v[116:119], %[xb1] offset:18432\n\t" \
    "v_mfma_f32_16x16x32_bf16 v[36:39], v[76:79], v[148:151], v[36:39]\n\t" \
    "ds_read_b128 v[120:123], %[xb1] offset:18560\n\t" \
    "s_waitcnt lgkmcnt(9)\n\t" \
    "v_mfma_f32_16x16x32_bf16 v[24:27], v[68:71], v[144:147], v[24:27]\n\t" \
    "ds_read_b64_tr_b16 v[140:141], %[wb0] offset:28672\n\t" \
    "ds_read_b64_tr_b16 v[142:143], %[wb0] offset:29184\n\t" \
    "v_mfma_f32_16x16x32_bf16 v[28:31], v[72:75], v[144:147], v[28:31]\n\t" \
    "ds_read_b128 v[124:127], %[xb1] offset:18688\n\t" \
    "ds_read_b128 v[128:131], %[xb1] offset:18816\n\t" \
    "v_mfma_f32_16x16x32_bf16 v[32:35], v[76:79], v[144:147], v[32:35]\n\t" \
    "buffer_load_dwordx4 v[40:43], %[l16], %[rs], %[ra] offen offset:0 sc1\n\t" \
    "buffer_load_dwordx4 v[44:47], %[l16], %[rs], %[ra] offen offset:1024 sc1\n\t" \
    "v_mfma_f32_16x16x32_bf16 v[36:39], v[80:83], v[144:147], v[36:39]\n\t" \
    "s_waitcnt lgkmcnt(9)\n\t" \
    "v_mfma_f32_16x16x32_bf16 v[24:27], v[72:75], v[152:155], v[24:27]\n\t" \
    "ds_read_b64_tr_b16 v[148:149], %[wb0] offset:32768\n\t" \
    "v_mfma_f32_16x16x32_bf16 v[28:31], v[76:79], v[152:155], v[28:31]\n\t" \
    "ds_read_b64_tr_b16 v[150:151], %[wb0] offset:33280\n\t" \
    "v_mfma_f32_16x16x32_bf16 v[32:35], v[80:83], v[152:155], v[32:35]\n\t" \
    "ds_read_b128 v[132:135], %[xb2] offset:18432\n\t" \
    "v_mfma_f32_16x16x32_bf16 v[36:39], v[84:87], v[152:155], v[36:39]\n\t" \
    "ds_read_b128 v[136:139], %[xb2] offset:18560\n\t" \
    "s_waitcnt lgkmcnt(9)\n\t" \
    "v_mfma_f32_16x16x32_bf16 v[24:27], v[76:79], v[156:159], v[24:27]\n\t" \
    "ds_read_b64_tr_b16 v[144:145], %[wb0] offset:36864\n\t" \
    "v_mfma_f32_16x16x32_bf16 v[28:31], v[80:83], v[156:159], v[28:31]\n\t" \
    "ds_read_b64_tr_b16 v[146:147], %[wb0] offset:37376\n\t" \
    "v_mfma_f32_16x16x32_bf16 v[32:35], v[84:87], v[156:159], v[32:35]\n\t" \
    "v_mfma_f32_16x16x32_bf16 v[36:39], v[88:91], v[156:159], v[36:39]\n\t" \
    "s_waitcnt lgkmcnt(9)\n\t" \
    "v_mfma_f32_16x16x32_bf16 v[24:27], v[80:83], v[160:163], v[24:27]\n\t" \
    "ds_read_b64_tr_b16 v[152:153], %[wb0] offset:40960\n\t" \
    "v_mfma_f32_16x16x32_bf16 v[28:31], v[84:87], v[160:163], v[28:31]\n\t" \
    "ds_read_b64_tr_b16 v[154:155], %[wb0] offset:41472\n\t" \
    "v_mfma_f32_16x16x32_bf16 v[32:35], v[88:91], v[160:163], v[32:35]\n\t" \
    "v_mfma_f32_16x16x32_bf16 v[36:39], v[92:95], v[160:163], v[36:39]\n\t" \
    "s_waitcnt lgkmcnt(9)\n\t" \
    "v_mfma_f32_16x16x32_bf16 v[24:27], v[84:87], v[140:143], v[24:27]\n\t" \
    "ds_read_b64_tr_b16 v[156:157], %[wb0] offset:45056\n\t" \
    "v_mfma_f32_16x16x32_bf16 v[28:31], v[88:91], v[140:143], v[28:31]\n\t" \
    "ds_read_b64_tr_b16 v[158:159], %[wb0] offset:45568\n\t" \
    "v_mfma_f32_16x16x32_bf16 v[32:35], v[92:95], v[140:143], v[32:35]\n\t" \
    "v_mfma_f32_16x16x32_bf16 v[36:39], v[96:99], v[140:143], v[36:39]\n\t" \
    "s_waitcnt lgkmcnt(8)\n\t" \
    "v_mfma_f32_16x16x32_bf16 v[24:27], v[100:103], v[148:151], v[24:27]\n\t" \
    "ds_read_b64_tr_b16 v[160:161], %[wb0] offset:49152\n\t" \
    "v_mfma_f32_16x16x32_bf16 v[28:31], v[104:107], v[148:151], v[28:31]\n\t" \
    "ds_read_b64_tr_b16 v[162:163], %[wb0] offset:49664\n\t" \
    "v_mfma_f32_16x16x32_bf16 v[32:35], v[108:111], v[148:151], v[32:35]\n\t" \
    "v_mfma_f32_16x16x32_bf16 v[36:39], v[112:115], v[148:151], v[36:39]\n\t" \
    "s_waitcnt lgkmcnt(6)\n\t" \
    "v_mfma_f32_16x16x32_bf16 v[24:27], v[104:107], v[144:147], v[24:27]\n\t" \
    "ds_read_b64_tr_b16 v[140:141], %[wb0] offset:53248\n\t" \
    "v_mfma_f32_16x16x32_bf16 v[28:31], v[108:111], v[144:147], v[28:31]\n\t" \
    "ds_read_b64_tr_b16 v[142:143], %[wb0] offset:53760\n\t" \
    "v_mfma_f32_16x16x32_bf16 v[32:35], v[112:115], v[144:147], v[32:35]\n\t" \
    "v_mfma_f32_16x16x32_bf16 v[36:39], v[116:119], v[144:147], v[36:39]\n\t" \
    "s_waitcnt lgkmcnt(6)\n\t" \
    "v_mfma_f32_16x16x32_bf16 v[24:27], v[108:111], v[152:155], v[24:27]\n\t" \
    "ds_read_b64_tr_b16 v[148:149], %[wb0] offset:57344\n\t" \
    "v_mfma_f32_16x16x32_bf16 v[28:31], v[112:115], v[152:155], v[28:31]\n\t" \
    "ds_read_b64_tr_b16 v[150:151], %[wb0] offset:57856\n\t" \
    "v_mfma_f32_16x16x32_bf16 v[32:35], v[116:119], v[152:155], v[32:35]\n\t" \
    "v_mfma_f32_16x16x32_bf16 v[36:39], v[120:123], v[152:155], v[36:39]\n\t" \
    "s_waitcnt lgkmcnt(6)\n\t" \
    "v_mfma_f32_16x16x32_bf16 v[24:27], v[112:115], v[156:159], v[24:27]\n\t" \
    "v_mfma_f32_16x16x32_bf16 v[28:31], v[116:119], v[156:159], v[28:31]\n\t" \
    "v_mfma_f32_16x16x32_bf16 v[32:35], v[120:123], v[156:159], v[32:35]\n\t" \
    "v_mfma_f32_16x16x32_bf16 v[36:39], v[124:127], v[156:159], v[36:39]\n\t" \
    "s_waitcnt lgkmcnt(4)\n\t" \
    "v_mfma_f32_16x16x32_bf16 v[24:27], v[116:119], v[160:163], v[24:27]\n\t" \
    "s_waitcnt vmcnt(0)\n\t" \
    "v_max3_u32 v12, v40, v41, v42\n\t" \
    "v_mfma_f32_16x16x32_bf16 v[28:31], v[120:123], v[160:163], v[28:31]\n\t" \
    "v_max3_u32 v13, v43, v44, v45\n\t" \
    "v_max3_u32 v12, v12, v46, v47\n\t" \
    "v_mfma_f32_16x16x32_bf16 v[32:35], v[124:127], v[160:163], v[32:35]\n\t" \
    "v_max_u32 v12, v12, v13\n\t" \
    "v_cmp_eq_u32 vcc, -1, v12\n\t" \
    "v_mfma_f32_16x16x32_bf16 v[36:39], v[128:131], v[160:163], v[36:39]\n\t" \
    "v_lshlrev_b32 v56, 16, v40\n\t" \
    "v_and_b32 v57, 0xffff0000, v40\n\t" \
    "s_waitcnt lgkmcnt(2)\n\t" \
    "v_mfma_f32_16x16x32_bf16 v[24:27], v[120:123], v[140:143], v[24:27]\n\t" \
    "v_lshlrev_b32 v58, 16, v41\n\t" \
    "v_and_b32 v59, 0xffff0000, v41\n\t" \
    "v_mfma_f32_16x16x32_bf16 v[28:31], v[124:127], v[140:143], v[28:31]\n\t" \
    "v_lshlrev_b32 v60, 16, v42\n\t" \
    "v_and_b32 v61, 0xffff0000, v42\n\t" \
    "v_mfma_f32_16x16x32_bf16 v[32:35], v[128:131], v[140:143], v[32:35]\n\t" \
    "v_lshlrev_b32 v62, 16, v43\n\t" \
    "v_and_b32 v63, 0xffff0000, v43\n\t" \
    "v_mfma_f32_16x16x32_bf16 v[36:39], v[132:135], v[140:143], v[36:39]\n\t" \
    "v_lshlrev_b32 v64, 16, v44\n\t" \
    "v_and_b32 v65, 0xffff0000, v44\n\t" \
    "s_waitcnt lgkmcnt(0)\n\t" \
    "v_mfma_f32_16x16x32_bf16 v[24:27], v[124:127], v[148:151], v[24:27]\n\t" \
    "v_lshlrev_b32 v66, 16, v45\n\t" \
    "v_and_b32 v67, 0xffff0000, v45\n\t" \
    "v_mfma_f32_16x16x32_bf16 v[28:31], v[128:131], v[148:151], v[28:31]\n\t" \
    "v_lshlrev_b32 v68, 16, v46\n\t" \
    "v_and_b32 v69, 0xffff0000, v46\n\t" \
    "v_mfma_f32_16x16x32_bf16 v[32:35], v[132:135], v[148:151], v[32:35]\n\t" \
    "v_lshlrev_b32 v70, 16, v47\n\t" \
    "v_and_b32 v71, 0xffff0000, v47\n\t" \
    "v_mfma_f32_16x16x32_bf16 v[36:39], v[136:139], v[148:151], v[36:39]\n\t" \
    "s_cbranch_vccz L_end_%=\n\t" \
    "L_chk_%=:\n\t" \
    "buffer_load_dwordx4 v[40:43], %[l16], %[rs], %[ra] offen offset:0 sc1\n\t" \
    "buffer_load_dwordx4 v[44:47], %[l16], %[rs], %[ra] offen offset:1024 sc1\n\t" \
    "s_sub_u32 %[cnt], %[cnt], 0x10000\n\t" \
    "s_waitcnt vmcnt(0)\n\t" \
    "v_max3_u32 v12, v40, v41, v42\n\t" \
    "v_max3_u32 v13, v43, v44, v45\n\t" \
    "v_max3_u32 v12, v12, v46, v47\n\t" \
    "v_max_u32 v12, v12, v13\n\t" \
    "v_cmp_eq_u32 vcc, -1, v12\n\t" \
    "v_lshlrev_b32 v56, 16, v40\n\t" \
    "v_and_b32 v57, 0xffff0000, v40\n\t" \
    "v_lshlrev_b32 v58, 16, v41\n\t" \
    "v_and_b32 v59, 0xffff0000, v41\n\t" \
    "v_lshlrev_b32 v60, 16, v42\n\t" \
    "v_and_b32 v61, 0xffff0000, v42\n\t" \
    "v_lshlrev_b32 v62, 16, v43\n\t" \
    "v_and_b32 v63, 0xffff0000, v43\n\t" \
    "v_lshlrev_b32 v64, 16, v44\n\t" \
    "v_and_b32 v65, 0xffff0000, v44\n\t" \
    "v_lshlrev_b32 v66, 16, v45\n\t" \
    "v_and_b32 v67, 0xffff0000, v45\n\t" \
    "v_lshlrev_b32 v68, 16, v46\n\t" \
    "v_and_b32 v69, 0xffff0000, v46\n\t" \
    "v_lshlrev_b32 v70, 16, v47\n\t" \
    "v_and_b32 v71, 0xffff0000, v47\n\t" \
    "s_cbranch_scc1 L_end_%=\n\t" \
    "s_cbranch_vccnz L_chk_%=\n\t" \
    "L_end_%=:\n\t" \
    "s_nop 2\n\t" \
    "v_pk_add_f32 v[24:25], v[24:25], v[56:57]\n\t" \
    "v_pk_add_f32 v[26:27], v[26:27], v[58:59]\n\t" \
    "v_pk_add_f32 v[28:29], v[28:29], v[60:61]\n\t" \
    "v_pk_add_f32 v[30:31], v[30:31], v[62:63]\n\t" \
    "v_pk_add_f32 v[32:33], v[32:33], v[64:65]\n\t" \
    "v_pk_add_f32 v[34:35], v[34:35], v[66:67]\n\t" \
    "v_pk_add_f32 v[36:37], v[36:37], v[68:69]\n\t" \
    "v_pk_add_f32 v[38:39], v[38:39], v[70:71]\n\t"
#define MAIN_LOOP_CLOBBERS "v8", "v9", "v10", "v11", "v12", "v13", "v14", "v15", "v40", "v41", "v42", "v43", "v44", "v45", "v46", "v47", "v48", "v49", "v50", "v51", "v52", "v53", "v54", "v55", "v56", "v57", "v58", "v59", "v60", "v61", "v62", "v63", "v64", "v65", "v66", "v67", "v68", "v69", "v70", "v71", "v72", "v73", "v74", "v75", "v76", "v77", "v78", "v79", "v80", "v81", "v82", "v83", "v84", "v85", "v86", "v87", "v88", "v89", "v90", "v91", "v92", "v93", "v94", "v95", "v96", "v97", "v98", "v99", "v100", "v101", "v102", "v103", "v104", "v105", "v106", "v107", "v108", "v109", "v110", "v111", "v112", "v113", "v114", "v115", "v116", "v117", "v118", "v119", "v120", "v121", "v122", "v123", "v124", "v125", "v126", "v127", "v128", "v129", "v130", "v131", "v132", "v133", "v134", "v135", "v136", "v137", "v138", "v139", "v140", "v141", "v142", "v143", "v144", "v145", "v146", "v147", "v148", "v149", "v150", "v151", "v152", "v153", "v154", "v155", "v156", "v157", "v158", "v159", "v160", "v161", "v162", "v163"
#define TAIL_BARRIERS 13

#define LOADER_ASM \
    "v_readfirstlane_b32 s44, %[tid]\n\t" \
    "s_getpc_b64 %[pc]\n\t" \
    "s_cmp_ge_u32 s44, 0x1c0\n\t" \
    "s_cbranch_scc1 L_end_%=\n\t" \
    "s_cmp_ge_u32 s44, 0x180\n\t" \
    "s_cbranch_scc1 L_touch_%=\n\t" \
    "s_cmp_lt_u32 s44, 0x80\n\t" \
    "s_cselect_b32 s45, 8, 0\n\t" \
    "s_load_dwordx2 s[36:37], %[ka], s45\n\t" \
    "s_lshr_b32 s45, %[bid], 1\n\t" \
    "s_xor_b32 s46, s45, %[bid]\n\t" \
    "s_bfe_i32 s46, s46, 0x10006\n\t" \
    "s_lshr_b32 s44, s44, 6\n\t" \
    "s_lshr_b32 s47, %[bid], 5\n\t" \
    "s_bfe_u32 s55, %[bid], 0x20003\n\t" \
    "s_and_b32 s56, %[bid], 1\n\t" \
    "s_add_u32 s49, s55, s47\n\t" \
    "s_xor_b32 s50, s46, 0x400\n\t" \
    "s_sub_u32 s50, s50, s46\n\t" \
    "s_cmp_ge_u32 s44, 2\n\t" \
    "s_cbranch_scc1 L_x_%=\n\t" \
    "s_lshl_b32 s54, s50, 8\n\t" \
    "s_lshl_b32 s53, s50, 7\n\t" \
    "s_and_b32 s48, s46, 0x1c0000\n\t" \
    "s_and_b32 s49, s49, 3\n\t" \
    "s_lshl_b32 s49, s49, 14\n\t" \
    "s_add_u32 s57, s48, s49\n\t" \
    "s_lshl_b32 s52, s44, 11\n\t" \
    "s_add_u32 m0, s52, 36864\n\t" \
    "s_lshl_b32 s51, s44, 13\n\t" \
    "s_lshl_b32 s48, s56, 16\n\t" \
    "s_or_b32 s51, s51, s48\n\t" \
    "s_lshl_b32 s48, s55, 7\n\t" \
    "s_or_b32 s51, s51, s48\n\t" \
    "s_movk_i32 s50, 0x200\n\t" \
    "s_mov_b32 s38, 0x1e0000\n\t" \
    "s_movk_i32 s52, 0xc00\n\t" \
    "s_mov_b32 s49, 3\n\t" \
    "s_branch L_go_%=\n\t" \
    "L_x_%=:\n\t" \
    "s_lshl_b32 s54, s50, 4\n\t" \
    "s_and_b32 s48, s46, 0x10000\n\t" \
    "s_and_b32 s49, s49, 2\n\t" \
    "s_lshl_b32 s49, s49, 6\n\t" \
    "s_add_u32 s57, s48, s49\n\t" \
    "s_lshr_b32 s58, s44, 2\n\t" \
    "s_and_b32 s59, s44, 1\n\t" \
    "s_mul_i32 s53, s58, 18432\n\t" \
    "s_mul_i32 s52, s59, 8704\n\t" \
    "s_add_u32 s53, s53, s52\n\t" \
    "s_add_u32 m0, s53, 122880\n\t" \
    "s_lshl_b32 s48, s59, 7\n\t" \
    "s_or_b32 s48, s48, s58\n\t" \
    "s_xor_b32 s48, s48, s46\n\t" \
    "s_and_b32 s52, s46, 141\n\t" \
    "s_add_u32 s48, s48, s52\n\t" \
    "s_and_b32 s60, s45, 1\n\t" \
    "s_lshl_b32 s60, s60, 3\n\t" \
    "s_add_u32 s60, s60, s47\n\t" \
    "s_lshl_b32 s51, s60, 8\n\t" \
    "s_add_u32 s51, s51, s48\n\t" \
    "s_lshl_b32 s51, s51, 1\n\t" \
    "s_or_b32 s51, s51, s56\n\t" \
    "s_lshl_b32 s51, s51, 8\n\t" \
    "s_bfe_u32 s61, %[bid], 0x10002\n\t" \
    "s_mul_i32 s48, s61, 0x1f4000\n\t" \
    "s_add_u32 s51, s51, s48\n\t" \
    "s_add_u32 s38, s48, 0x1f4000\n\t" \
    "s_movk_i32 s52, 0x480\n\t" \
    "s_mov_b32 s49, 4\n\t" \
    "L_go_%=:\n\t" \
    "v_and_b32 v3, 7, %[tid]\n\t" \
    "v_bfe_u32 v2, %[tid], 3, 3\n\t" \
    "v_lshrrev_b32 v4, s49, %[tid]\n\t" \
    "v_and_b32 v4, 2, v4\n\t" \
    "v_xor_b32 v3, v3, v4\n\t" \
    "v_mul_i32_i24 v2, s50, v2\n\t" \
    "v_lshl_add_u32 v2, v3, 4, v2\n\t" \
    "s_lshl_b32 s50, s50, 3\n\t" \
    "v_add_u32 v2, s51, v2\n\t" \
    "s_mov_b32 s39, 0x20000\n\t" \
    "v_xad_u32 v3, v2, 64, s50\n\t" \
    "s_mov_b32 s48, s57\n\t" \
    "s_movk_i32 s49, 0x400\n\t" \
    "s_waitcnt lgkmcnt(0)\n\t" \
    "buffer_load_dwordx4 v2, s[36:39], s48 offen lds\n\t" \
    "s_add_u32 m0, m0, s49\n\t" \
    "s_nop 0\n\t" \
    "buffer_load_dwordx4 v3, s[36:39], s48 offen lds\n\t" \
    "s_add_u32 m0, m0, s52\n\t" \
    "s_add_u32 s48, s48, s54\n\t" \
    "buffer_load_dwordx4 v2, s[36:39], s48 offen lds\n\t" \
    "s_add_u32 m0, m0, s49\n\t" \
    "s_nop 0\n\t" \
    "buffer_load_dwordx4 v3, s[36:39], s48 offen lds\n\t" \
    "s_add_u32 m0, m0, s52\n\t" \
    "s_add_u32 s48, s48, s54\n\t" \
    "buffer_load_dwordx4 v2, s[36:39], s48 offen lds\n\t" \
    "s_add_u32 m0, m0, s49\n\t" \
    "s_nop 0\n\t" \
    "buffer_load_dwordx4 v3, s[36:39], s48 offen lds\n\t" \
    "s_add_u32 m0, m0, s52\n\t" \
    "s_add_u32 s48, s48, s54\n\t" \
    "buffer_load_dwordx4 v2, s[36:39], s48 offen lds\n\t" \
    "s_add_u32 m0, m0, s49\n\t" \
    "s_nop 0\n\t" \
    "buffer_load_dwordx4 v3, s[36:39], s48 offen lds\n\t" \
    "s_add_u32 m0, m0, s52\n\t" \
    "s_add_u32 s48, s48, s54\n\t" \
    "s_cmp_lt_u32 s44, 2\n\t" \
    "s_cbranch_scc1 L_w_%=\n\t" \
    "s_cmp_eq_u32 s59, 0\n\t" \
    "s_cbranch_scc1 L_x9_%=\n\t" \
    "buffer_load_dwordx4 v2, s[36:39], s48 offen lds\n\t" \
    "L_x9_%=:\n\t" \
    "s_setprio 0\n\t" \
    "s_load_dwordx2 s[40:41], %[ka], 0x30\n\t" \
    "s_cmp_eq_u32 s58, 0\n\t" \
    "s_cbranch_scc0 L_xp_%=\n\t" \
    "s_waitcnt vmcnt(0)\n\t" \
    "L_xp_%=:\n\t" \
    "s_barrier\n\t" \
    "s_barrier\n\t" \
    "s_barrier\n\t" \
    "s_xor_b32 s48, s57, 0x80\n\t" \
    "s_mov_b32 m0, s53\n\t" \
    "s_mov_b32 s42, 3145728\n\t" \
    "s_mov_b32 s43, 0x20000\n\t" \
    "s_lshl_b32 s62, %[bid], 2\n\t" \
    "s_add_u32 s62, s62, s44\n\t" \
    "s_lshl_b32 s62, s62, 11\n\t" \
    "s_add_u32 s62, s62, 1044480\n\t" \
    "s_barrier\n\t" \
    "s_lshl_b32 s63, s61, 2\n\t" \
    "s_add_u32 s63, s63, s55\n\t" \
    "s_lshl_b32 s63, s63, 1\n\t" \
    "s_add_u32 s63, s63, s56\n\t" \
    "s_lshl_b32 s63, s63, 1\n\t" \
    "s_add_u32 s63, s63, s59\n\t" \
    "s_lshl_b32 s63, s63, 4\n\t" \
    "s_add_u32 s63, s63, s60\n\t" \
    "s_waitcnt vmcnt(0)\n\t" \
    "s_barrier\n\t" \
    "s_lshl_b32 s63, s63, 1\n\t" \
    "s_add_u32 s63, s63, s58\n\t" \
    "s_lshl_b32 s63, s63, 7\n\t" \
    "s_add_u32 s63, s63, 4096\n\t" \
    "s_lshl_b32 s64, %[bid], 7\n\t" \
    "s_add_u32 s64, s64, 524288\n\t" \
    "v_and_b32 v8, 63, %[tid]\n\t" \
    "v_lshlrev_b32 v8, 4, v8\n\t" \
    "s_barrier\n\t" \
    "v_mov_b64 v[4:5], -1\n\t" \
    "v_mov_b64 v[6:7], -1\n\t" \
    "v_mov_b32 v9, 160512\n\t" \
    "s_waitcnt lgkmcnt(0)\n\t" \
    "s_barrier\n\t" \
    "v_and_b32 v12, 8, %[tid]\n\t" \
    "v_lshrrev_b32 v12, 2, v12\n\t" \
    "v_and_b32 v13, 7, %[tid]\n\t" \
    "v_lshl_or_b32 v12, v13, 5, v12\n\t" \
    "s_and_b32 s66, %[bid], 0x1d\n\t" \
    "v_or_b32 v12, s66, v12\n\t" \
    "s_barrier\n\t" \
    "s_xor_b32 s66, %[bid], 1\n\t" \
    "v_mov_b32 v13, s66\n\t" \
    "s_mov_b64 exec, 0xffff\n\t" \
    "v_mov_b32 v13, v12\n\t" \
    "s_mov_b64 exec, -1\n\t" \
    "v_lshlrev_b32 v12, 7, v13\n\t" \
    "s_mov_b32 s66, 524288\n\t" \
    "s_mov_b32 s67, 0x10000\n\t" \
    "s_barrier\n\t" \
    "s_barrier\n\t" \
    "s_barrier\n\t" \
    "buffer_load_dwordx4 v2, s[36:39], s48 offen lds\n\t" \
    "s_add_u32 m0, m0, s49\n\t" \
    "s_nop 0\n\t" \
    "s_barrier\n\t" \
    "buffer_load_dwordx4 v3, s[36:39], s48 offen lds\n\t" \
    "s_add_u32 m0, m0, s52\n\t" \
    "s_add_u32 s48, s48, s54\n\t" \
    "s_barrier\n\t" \
    "buffer_load_dwordx4 v2, s[36:39], s48 offen lds\n\t" \
    "s_add_u32 m0, m0, s49\n\t" \
    "s_nop 0\n\t" \
    "s_barrier\n\t" \
    "buffer_load_dwordx4 v3, s[36:39], s48 offen lds\n\t" \
    "s_add_u32 m0, m0, s52\n\t" \
    "s_add_u32 s48, s48, s54\n\t" \
    "s_barrier\n\t" \
    "buffer_load_dwordx4 v2, s[36:39], s48 offen lds\n\t" \
    "s_add_u32 m0, m0, s49\n\t" \
    "s_nop 0\n\t" \
    "s_barrier\n\t" \
    "buffer_load_dwordx4 v3, s[36:39], s48 offen lds\n\t" \
    "s_add_u32 m0, m0, s52\n\t" \
    "s_add_u32 s48, s48, s54\n\t" \
    "s_barrier\n\t" \
    "buffer_load_dwordx4 v2, s[36:39], s48 offen lds\n\t" \
    "s_add_u32 m0, m0, s49\n\t" \
    "s_nop 0\n\t" \
    "s_barrier\n\t" \
    "buffer_load_dwordx4 v3, s[36:39], s48 offen lds\n\t" \
    "s_add_u32 m0, m0, s52\n\t" \
    "s_add_u32 s48, s48, s54\n\t" \
    "s_barrier\n\t" \
    "buffer_load_dwordx4 v2, s[36:39], s48 offen lds\n\t" \
    "s_barrier\n\t" \
    "buffer_store_dwordx4 v[4:7], v8, s[40:43], s62 offen sc1\n\t" \
    "s_barrier\n\t" \
    "buffer_store_dwordx4 v[4:7], v8, s[40:43], s62 offen offset:1024 sc1\n\t" \
    "s_barrier\n\t" \
    "s_mov_b64 exec, 0xff\n\t" \
    "buffer_store_dwordx4 v[4:7], v8, s[40:43], s63 offen sc1\n\t" \
    "s_mov_b64 exec, -1\n\t" \
    "s_barrier\n\t" \
    "s_barrier\n\t" \
    "s_barrier\n\t" \
    "s_barrier\n\t" \
    "s_waitcnt vmcnt(3)\n\t" \
    "s_barrier\n\t" \
    "s_barrier\n\t" \
    "s_barrier\n\t" \
    "s_barrier\n\t" \
    "s_barrier\n\t" \
    "s_barrier\n\t" \
    "s_barrier\n\t" \
    "s_barrier\n\t" \
    "ds_read_b32 v10, v9\n\t" \
    "s_barrier\n\t" \
    "s_barrier\n\t" \
    "s_waitcnt lgkmcnt(0)\n\t" \
    "v_add_u32 v11, 2, v10\n\t" \
    "v_add_u32 v10, 1, v10\n\t" \
    "s_barrier\n\t" \
    "s_waitcnt vmcnt(0)\n\t" \
    "s_cmp_lt_u32 s44, 4\n\t" \
    "s_barrier\n\t" \
    "s_cbranch_scc1 L_xk_%=\n\t" \
    "s_endpgm\n\t" \
    "L_xk_%=:\n\t" \
    "s_cmp_eq_u32 s44, 3\n\t" \
    "s_cbranch_scc0 L_xf_%=\n\t" \
    "s_mov_b64 exec, 1\n\t" \
    "buffer_store_dwordx2 v[10:11], off, s[40:43], s64 sc1\n\t" \
    "s_mov_b64 exec, -1\n\t" \
    "L_xf_%=:\n\t" \
    "s_barrier\n\t" \
    "s_barrier\n\t" \
    "s_barrier\n\t" \
    "s_barrier\n\t" \
    "s_barrier\n\t" \
    "s_cmp_eq_u32 s44, 2\n\t" \
    "s_cbranch_scc0 L_fl_%=\n\t" \
    "buffer_load_dwordx2 v[14:15], v12, s[40:43], s66 offen sc1\n\t" \
    "L_fl_%=:\n\t" \
    "s_barrier\n\t" \
    "s_cbranch_scc0 L_fb_%=\n\t" \
    "L_fc_%=:\n\t" \
    "s_waitcnt vmcnt(0)\n\t" \
    "v_cmp_ne_u64 vcc, v[14:15], v[10:11]\n\t" \
    "s_sub_u32 s67, s67, 1\n\t" \
    "s_cbranch_vccz L_fb_%=\n\t" \
    "s_cbranch_scc1 L_fb_%=\n\t" \
    "buffer_load_dwordx2 v[14:15], v12, s[40:43], s66 offen sc1\n\t" \
    "s_branch L_fc_%=\n\t" \
    "L_touch_%=:\n\t" \
    "s_branch L_end_%=\n\t" \
    "L_w_%=:\n\t" \
    "s_xor_b32 s58, s57, 0x4000\n\t" \
    "s_xor_b32 s59, s57, 0x8000\n\t" \
    "s_xor_b32 s60, s57, 0xc000\n\t" \
    "buffer_load_dwordx4 v2, s[36:39], s48 offen lds\n\t" \
    "s_add_u32 m0, m0, 0x400\n\t" \
    "s_nop 0\n\t" \
    "buffer_load_dwordx4 v3, s[36:39], s48 offen lds\n\t" \
    "s_add_u32 m0, m0, 0xc00\n\t" \
    "s_add_u32 s48, s48, s54\n\t" \
    "s_waitcnt vmcnt(6)\n\t" \
    "s_barrier\n\t" \
    "buffer_load_dwordx4 v2, s[36:39], s48 offen lds\n\t" \
    "s_add_u32 m0, m0, 0x400\n\t" \
    "s_nop 0\n\t" \
    "buffer_load_dwordx4 v3, s[36:39], s48 offen lds\n\t" \
    "s_add_u32 m0, m0, 0xc00\n\t" \
    "s_add_u32 s48, s48, s54\n\t" \
    "buffer_load_dwordx4 v2, s[36:39], s48 offen lds\n\t" \
    "s_add_u32 m0, m0, 0x400\n\t" \
    "s_nop 0\n\t" \
    "buffer_load_dwordx4 v3, s[36:39], s48 offen lds\n\t" \
    "s_add_u32 m0, m0, 0xc00\n\t" \
    "s_add_u32 s48, s48, s54\n\t" \
    "s_waitcnt vmcnt(8)\n\t" \
    "s_barrier\n\t" \
    "buffer_load_dwordx4 v2, s[36:39], s48 offen lds\n\t" \
    "s_add_u32 m0, m0, 0x400\n\t" \
    "s_mul_i32 s49, s53, 1\n\t" \
    "buffer_load_dwordx4 v3, s[36:39], s48 offen lds\n\t" \
    "s_add_u32 m0, m0, 0xc00\n\t" \
    "s_add_u32 s48, s49, s57\n\t" \
    "buffer_load_dwordx4 v2, s[36:39], s48 offen lds\n\t" \
    "s_add_u32 m0, m0, 0x400\n\t" \
    "s_nop 0\n\t" \
    "buffer_load_dwordx4 v3, s[36:39], s48 offen lds\n\t" \
    "s_add_u32 m0, m0, 0xc00\n\t" \
    "s_add_u32 s48, s48, s54\n\t" \
    "s_waitcnt vmcnt(10)\n\t" \
    "s_barrier\n\t" \
    "buffer_load_dwordx4 v2, s[36:39], s48 offen lds\n\t" \
    "s_add_u32 m0, m0, 0x400\n\t" \
    "s_nop 0\n\t" \
    "buffer_load_dwordx4 v3, s[36:39], s48 offen lds\n\t" \
    "s_add_u32 m0, m0, 0xc00\n\t" \
    "s_add_u32 s48, s48, s54\n\t" \
    "buffer_load_dwordx4 v2, s[36:39], s48 offen lds\n\t" \
    "s_add_u32 m0, m0, 0x400\n\t" \
    "s_nop 0\n\t" \
    "buffer_load_dwordx4 v3, s[36:39], s48 offen lds\n\t" \
    "s_add_u32 m0, m0, 0xc00\n\t" \
    "s_add_u32 s48, s48, s54\n\t" \
    "s_waitcnt vmcnt(12)\n\t" \
    "s_barrier\n\t" \
    "buffer_load_dwordx4 v2, s[36:39], s48 offen lds\n\t" \
    "s_add_u32 m0, m0, 0x400\n\t" \
    "s_nop 0\n\t" \
    "buffer_load_dwordx4 v3, s[36:39], s48 offen lds\n\t" \
    "s_add_u32 m0, m0, 0xc00\n\t" \
    "s_add_u32 s48, s48, s54\n\t" \
    "buffer_load_dwordx4 v2, s[36:39], s48 offen lds\n\t" \
    "s_add_u32 m0, m0, 0x400\n\t" \
    "s_nop 0\n\t" \
    "buffer_load_dwordx4 v3, s[36:39], s48 offen lds\n\t" \
    "s_add_u32 m0, m0, 0xc00\n\t" \
    "s_add_u32 s48, s48, s54\n\t" \
    "s_waitcnt vmcnt(14)\n\t" \
    "s_barrier\n\t" \
    "buffer_load_dwordx4 v2, s[36:39], s48 offen lds\n\t" \
    "s_add_u32 m0, m0, 0x400\n\t" \
    "s_nop 0\n\t" \
    "buffer_load_dwordx4 v3, s[36:39], s48 offen lds\n\t" \
    "s_add_u32 m0, m0, 0xc00\n\t" \
    "s_add_u32 s48, s48, s54\n\t" \
    "buffer_load_dwordx4 v2, s[36:39], s48 offen lds\n\t" \
    "s_add_u32 m0, m0, 0x400\n\t" \
    "s_mul_i32 s49, s53, 0\n\t" \
    "buffer_load_dwordx4 v3, s[36:39], s48 offen lds\n\t" \
    "s_add_u32 m0, m0, 0xffff1c00\n\t" \
    "s_add_u32 s48, s49, s58\n\t" \
    "s_waitcnt vmcnt(16)\n\t" \
    "s_barrier\n\t" \
    "buffer_load_dwordx4 v2, s[36:39], s48 offen lds\n\t" \
    "s_add_u32 m0, m0, 0x400\n\t" \
    "s_nop 0\n\t" \
    "buffer_load_dwordx4 v3, s[36:39], s48 offen lds\n\t" \
    "s_add_u32 m0, m0, 0xc00\n\t" \
    "s_add_u32 s48, s48, s54\n\t" \
    "buffer_load_dwordx4 v2, s[36:39], s48 offen lds\n\t" \
    "s_add_u32 m0, m0, 0x400\n\t" \
    "s_nop 0\n\t" \
    "buffer_load_dwordx4 v3, s[36:39], s48 offen lds\n\t" \
    "s_add_u32 m0, m0, 0xc00\n\t" \
    "s_add_u32 s48, s48, s54\n\t" \
    "s_waitcnt vmcnt(18)\n\t" \
    "s_barrier\n\t" \
    "buffer_load_dwordx4 v2, s[36:39], s48 offen lds\n\t" \
    "s_add_u32 m0, m0, 0x400\n\t" \
    "s_nop 0\n\t" \
    "buffer_load_dwordx4 v3, s[36:39], s48 offen lds\n\t" \
    "s_add_u32 m0, m0, 0xc00\n\t" \
    "s_add_u32 s48, s48, s54\n\t" \
    "buffer_load_dwordx4 v2, s[36:39], s48 offen lds\n\t" \
    "s_add_u32 m0, m0, 0x400\n\t" \
    "s_nop 0\n\t" \
    "buffer_load_dwordx4 v3, s[36:39], s48 offen lds\n\t" \
    "s_add_u32 m0, m0, 0xc00\n\t" \
    "s_add_u32 s48, s48, s54\n\t" \
    "s_waitcnt vmcnt(20)\n\t" \
    "s_barrier\n\t" \
    "buffer_load_dwordx4 v2, s[36:39], s48 offen lds\n\t" \
    "s_add_u32 m0, m0, 0x400\n\t" \
    "s_nop 0\n\t" \
    "buffer_load_dwordx4 v3, s[36:39], s48 offen lds\n\t" \
    "s_add_u32 m0, m0, 0xc00\n\t" \
    "s_add_u32 s48, s48, s54\n\t" \
    "buffer_load_dwordx4 v2, s[36:39], s48 offen lds\n\t" \
    "s_add_u32 m0, m0, 0x400\n\t" \
    "s_nop 0\n\t" \
    "buffer_load_dwordx4 v3, s[36:39], s48 offen lds\n\t" \
    "s_add_u32 m0, m0, 0xc00\n\t" \
    "s_add_u32 s48, s48, s54\n\t" \
    "s_waitcnt vmcnt(22)\n\t" \
    "s_barrier\n\t" \
    "buffer_load_dwordx4 v2, s[36:39], s48 offen lds\n\t" \
    "s_add_u32 m0, m0, 0x400\n\t" \
    "s_nop 0\n\t" \
    "buffer_load_dwordx4 v3, s[36:39], s48 offen lds\n\t" \
    "s_add_u32 m0, m0, 0xc00\n\t" \
    "s_add_u32 s48, s48, s54\n\t" \
    "buffer_load_dwordx4 v2, s[36:39], s48 offen lds\n\t" \
    "s_add_u32 m0, m0, 0x400\n\t" \
    "s_mul_i32 s49, s53, 1\n\t" \
    "buffer_load_dwordx4 v3, s[36:39], s48 offen lds\n\t" \
    "s_add_u32 m0, m0, 0xc00\n\t" \
    "s_add_u32 s48, s49, s58\n\t" \
    "s_waitcnt vmcnt(24)\n\t" \
    "s_barrier\n\t" \
    "buffer_load_dwordx4 v2, s[36:39], s48 offen lds\n\t" \
    "s_add_u32 m0, m0, 0x400\n\t" \
    "s_nop 0\n\t" \
    "buffer_load_dwordx4 v3, s[36:39], s48 offen lds\n\t" \
    "s_add_u32 m0, m0, 0xc00\n\t" \
    "s_add_u32 s48, s48, s54\n\t" \
    "s_waitcnt vmcnt(24)\n\t" \
    "s_barrier\n\t" \
    "buffer_load_dwordx4 v2, s[36:39], s48 offen lds\n\t" \
    "s_add_u32 m0, m0, 0x400\n\t" \
    "s_nop 0\n\t" \
    "buffer_load_dwordx4 v3, s[36:39], s48 offen lds\n\t" \
    "s_add_u32 m0, m0, 0xc00\n\t" \
    "s_add_u32 s48, s48, s54\n\t" \
    "s_waitcnt vmcnt(24)\n\t" \
    "s_barrier\n\t" \
    "buffer_load_dwordx4 v2, s[36:39], s48 offen lds\n\t" \
    "s_add_u32 m0, m0, 0x400\n\t" \
    "s_nop 0\n\t" \
    "buffer_load_dwordx4 v3, s[36:39], s48 offen lds\n\t" \
    "s_add_u32 m0, m0, 0xc00\n\t" \
    "s_add_u32 s48, s48, s54\n\t" \
    "s_waitcnt vmcnt(24)\n\t" \
    "s_barrier\n\t" \
    "buffer_load_dwordx4 v2, s[36:39], s48 offen lds\n\t" \
    "s_add_u32 m0, m0, 0x400\n\t" \
    "s_nop 0\n\t" \
    "buffer_load_dwordx4 v3, s[36:39], s48 offen lds\n\t" \
    "s_add_u32 m0, m0, 0xc00\n\t" \
    "s_add_u32 s48, s48, s54\n\t" \
    "s_waitcnt vmcnt(24)\n\t" \
    "s_barrier\n\t" \
    "buffer_load_dwordx4 v2, s[36:39], s48 offen lds\n\t" \
    "s_add_u32 m0, m0, 0x400\n\t" \
    "s_nop 0\n\t" \
    "buffer_load_dwordx4 v3, s[36:39], s48 offen lds\n\t" \
    "s_add_u32 m0, m0, 0xc00\n\t" \
    "s_add_u32 s48, s48, s54\n\t" \
    "s_waitcnt vmcnt(24)\n\t" \
    "s_barrier\n\t" \
    "buffer_load_dwordx4 v2, s[36:39], s48 offen lds\n\t" \
    "s_add_u32 m0, m0, 0x400\n\t" \
    "s_nop 0\n\t" \
    "buffer_load_dwordx4 v3, s[36:39], s48 offen lds\n\t" \
    "s_add_u32 m0, m0, 0xc00\n\t" \
    "s_add_u32 s48, s48, s54\n\t" \
    "s_waitcnt vmcnt(24)\n\t" \
    "s_barrier\n\t" \
    "buffer_load_dwordx4 v2, s[36:39], s48 offen lds\n\t" \
    "s_add_u32 m0, m0, 0x400\n\t" \
    "s_mul_i32 s49, s53, 0\n\t" \
    "buffer_load_dwordx4 v3, s[36:39], s48 offen lds\n\t" \
    "s_add_u32 m0, m0, 0xffff1c00\n\t" \
    "s_add_u32 s48, s49, s59\n\t" \
    "s_waitcnt vmcnt(24)\n\t" \
    "s_barrier\n\t" \
    "buffer_load_dwordx4 v2, s[36:39], s48 offen lds\n\t" \
    "s_add_u32 m0, m0, 0x400\n\t" \
    "s_nop 0\n\t" \
    "buffer_load_dwordx4 v3, s[36:39], s48 offen lds\n\t" \
    "s_add_u32 m0, m0, 0xc00\n\t" \
    "s_add_u32 s48, s48, s54\n\t" \
    "s_waitcnt vmcnt(24)\n\t" \
    "s_barrier\n\t" \
    "buffer_load_dwordx4 v2, s[36:39], s48 offen lds\n\t" \
    "s_add_u32 m0, m0, 0x400\n\t" \
    "s_nop 0\n\t" \
    "buffer_load_dwordx4 v3, s[36:39], s48 offen lds\n\t" \
    "s_add_u32 m0, m0, 0xc00\n\t" \
    "s_add_u32 s48, s48, s54\n\t" \
    "s_waitcnt vmcnt(24)\n\t" \
    "s_barrier\n\t" \
    "buffer_load_dwordx4 v2, s[36:39], s48 offen lds\n\t" \
    "s_add_u32 m0, m0, 0x400\n\t" \
    "s_nop 0\n\t" \
    "buffer_load_dwordx4 v3, s[36:39], s48 offen lds\n\t" \
    "s_add_u32 m0, m0, 0xc00\n\t" \
    "s_add_u32 s48, s48, s54\n\t" \
    "s_waitcnt vmcnt(24)\n\t" \
    "s_barrier\n\t" \
    "s_waitcnt vmcnt(22)\n\t" \
    "s_barrier\n\t" \
    "s_waitcnt vmcnt(20)\n\t" \
    "s_barrier\n\t" \
    "s_waitcnt vmcnt(18)\n\t" \
    "s_barrier\n\t" \
    "s_waitcnt vmcnt(16)\n\t" \
    "s_barrier\n\t" \
    "buffer_load_dwordx4 v2, s[36:39], s48 offen lds\n\t" \
    "s_add_u32 m0, m0, 0x400\n\t" \
    "s_nop 0\n\t" \
    "buffer_load_dwordx4 v3, s[36:39], s48 offen lds\n\t" \
    "s_add_u32 m0, m0, 0xc00\n\t" \
    "s_add_u32 s48, s48, s54\n\t" \
    "buffer_load_dwordx4 v2, s[36:39], s48 offen lds\n\t" \
    "s_add_u32 m0, m0, 0x400\n\t" \
    "s_nop 0\n\t" \
    "buffer_load_dwordx4 v3, s[36:39], s48 offen lds\n\t" \
    "s_add_u32 m0, m0, 0xc00\n\t" \
    "s_add_u32 s48, s48, s54\n\t" \
    "s_waitcnt vmcnt(18)\n\t" \
    "s_barrier\n\t" \
    "buffer_load_dwordx4 v2, s[36:39], s48 offen lds\n\t" \
    "s_add_u32 m0, m0, 0x400\n\t" \
    "s_nop 0\n\t" \
    "buffer_load_dwordx4 v3, s[36:39], s48 offen lds\n\t" \
    "s_add_u32 m0, m0, 0xc00\n\t" \
    "s_add_u32 s48, s48, s54\n\t" \
    "buffer_load_dwordx4 v2, s[36:39], s48 offen lds\n\t" \
    "s_add_u32 m0, m0, 0x400\n\t" \
    "s_nop 0\n\t" \
    "buffer_load_dwordx4 v3, s[36:39], s48 offen lds\n\t" \
    "s_add_u32 m0, m0, 0xc00\n\t" \
    "s_add_u32 s48, s48, s54\n\t" \
    "s_waitcnt vmcnt(20)\n\t" \
    "s_barrier\n\t" \
    "buffer_load_dwordx4 v2, s[36:39], s48 offen lds\n\t" \
    "s_add_u32 m0, m0, 0x400\n\t" \
    "s_mul_i32 s49, s53, 1\n\t" \
    "buffer_load_dwordx4 v3, s[36:39], s48 offen lds\n\t" \
    "s_add_u32 m0, m0, 0xc00\n\t" \
    "s_add_u32 s48, s49, s59\n\t" \
    "buffer_load_dwordx4 v2, s[36:39], s48 offen lds\n\t" \
    "s_add_u32 m0, m0, 0x400\n\t" \
    "s_nop 0\n\t" \
    "buffer_load_dwordx4 v3, s[36:39], s48 offen lds\n\t" \
    "s_add_u32 m0, m0, 0xc00\n\t" \
    "s_add_u32 s48, s48, s54\n\t" \
    "s_waitcnt vmcnt(22)\n\t" \
    "s_barrier\n\t" \
    "buffer_load_dwordx4 v2, s[36:39], s48 offen lds\n\t" \
    "s_add_u32 m0, m0, 0x400\n\t" \
    "s_nop 0\n\t" \
    "buffer_load_dwordx4 v3, s[36:39], s48 offen lds\n\t" \
    "s_add_u32 m0, m0, 0xc00\n\t" \
    "s_add_u32 s48, s48, s54\n\t" \
    "buffer_load_dwordx4 v2, s[36:39], s48 offen lds\n\t" \
    "s_add_u32 m0, m0, 0x400\n\t" \
    "s_nop 0\n\t" \
    "buffer_load_dwordx4 v3, s[36:39], s48 offen lds\n\t" \
    "s_add_u32 m0, m0, 0xc00\n\t" \
    "s_add_u32 s48, s48, s54\n\t" \
    "s_waitcnt vmcnt(24)\n\t" \
    "s_barrier\n\t" \
    "buffer_load_dwordx4 v2, s[36:39], s48 offen lds\n\t" \
    "s_add_u32 m0, m0, 0x400\n\t" \
    "s_mul_i32 s49, s53, 0\n\t" \
    "buffer_load_dwordx4 v3, s[36:39], s48 offen lds\n\t" \
    "s_add_u32 m0, m0, 0x3c00\n\t" \
    "s_add_u32 s48, s49, s60\n\t" \
    "buffer_load_dwordx4 v2, s[36:39], s48 offen lds\n\t" \
    "s_add_u32 m0, m0, 0x400\n\t" \
    "s_mul_i32 s49, s53, 9\n\t" \
    "buffer_load_dwordx4 v3, s[36:39], s48 offen lds\n\t" \
    "s_add_u32 m0, m0, 0xffffcc00\n\t" \
    "s_add_u32 s48, s49, s59\n\t" \
    "s_waitcnt vmcnt(26)\n\t" \
    "s_barrier\n\t" \
    "buffer_load_dwordx4 v2, s[36:39], s48 offen lds\n\t" \
    "s_add_u32 m0, m0, 0x400\n\t" \
    "s_mul_i32 s49, s53, 2\n\t" \
    "buffer_load_dwordx4 v3, s[36:39], s48 offen lds\n\t" \
    "s_add_u32 m0, m0, 0x3c00\n\t" \
    "s_add_u32 s48, s49, s60\n\t" \
    "buffer_load_dwordx4 v2, s[36:39], s48 offen lds\n\t" \
    "s_add_u32 m0, m0, 0x400\n\t" \
    "s_mul_i32 s49, s53, 11\n\t" \
    "buffer_load_dwordx4 v3, s[36:39], s48 offen lds\n\t" \
    "s_add_u32 m0, m0, 0xffffcc00\n\t" \
    "s_add_u32 s48, s49, s59\n\t" \
    "s_waitcnt vmcnt(28)\n\t" \
    "s_barrier\n\t" \
    "buffer_load_dwordx4 v2, s[36:39], s48 offen lds\n\t" \
    "s_add_u32 m0, m0, 0x400\n\t" \
    "s_mul_i32 s49, s53, 4\n\t" \
    "buffer_load_dwordx4 v3, s[36:39], s48 offen lds\n\t" \
    "s_add_u32 m0, m0, 0x3c00\n\t" \
    "s_add_u32 s48, s49, s60\n\t" \
    "buffer_load_dwordx4 v2, s[36:39], s48 offen lds\n\t" \
    "s_add_u32 m0, m0, 0x400\n\t" \
    "s_mul_i32 s49, s53, 13\n\t" \
    "buffer_load_dwordx4 v3, s[36:39], s48 offen lds\n\t" \
    "s_add_u32 m0, m0, 0xffffcc00\n\t" \
    "s_add_u32 s48, s49, s59\n\t" \
    "s_waitcnt vmcnt(30)\n\t" \
    "s_barrier\n\t" \
    "buffer_load_dwordx4 v2, s[36:39], s48 offen lds\n\t" \
    "s_add_u32 m0, m0, 0x400\n\t" \
    "s_mul_i32 s49, s53, 6\n\t" \
    "buffer_load_dwordx4 v3, s[36:39], s48 offen lds\n\t" \
    "s_add_u32 m0, m0, 0x3c00\n\t" \
    "s_add_u32 s48, s49, s60\n\t" \
    "buffer_load_dwordx4 v2, s[36:39], s48 offen lds\n\t" \
    "s_add_u32 m0, m0, 0x400\n\t" \
    "s_mul_i32 s49, s53, 12\n\t" \
    "buffer_load_dwordx4 v3, s[36:39], s48 offen lds\n\t" \
    "s_add_u32 m0, m0, 0x2c00\n\t" \
    "s_add_u32 s48, s49, s60\n\t" \
    "buffer_load_dwordx4 v2, s[36:39], s48 offen lds\n\t" \
    "s_add_u32 m0, m0, 0x400\n\t" \
    "s_mul_i32 s49, s53, 8\n\t" \
    "buffer_load_dwordx4 v3, s[36:39], s48 offen lds\n\t" \
    "s_add_u32 m0, m0, 0xffffdc00\n\t" \
    "s_add_u32 s48, s49, s60\n\t" \
    "s_waitcnt vmcnt(28)\n\t" \
    "s_barrier\n\t" \
    "buffer_load_dwordx4 v2, s[36:39], s48 offen lds\n\t" \
    "s_add_u32 m0, m0, 0x400\n\t" \
    "s_mul_i32 s49, s53, 14\n\t" \
    "buffer_load_dwordx4 v3, s[36:39], s48 offen lds\n\t" \
    "s_add_u32 m0, m0, 0x2c00\n\t" \
    "s_add_u32 s48, s49, s60\n\t" \
    "buffer_load_dwordx4 v2, s[36:39], s48 offen lds\n\t" \
    "s_add_u32 m0, m0, 0x400\n\t" \
    "s_mul_i32 s49, s53, 1\n\t" \
    "buffer_load_dwordx4 v3, s[36:39], s48 offen lds\n\t" \
    "s_add_u32 m0, m0, 0xc00\n\t" \
    "s_add_u32 s48, s49, s60\n\t" \
    "buffer_load_dwordx4 v2, s[36:39], s48 offen lds\n\t" \
    "s_add_u32 m0, m0, 0x400\n\t" \
    "s_mul_i32 s49, s53, 10\n\t" \
    "buffer_load_dwordx4 v3, s[36:39], s48 offen lds\n\t" \
    "s_add_u32 m0, m0, 0xffffcc00\n\t" \
    "s_add_u32 s48, s49, s60\n\t" \
    "s_waitcnt vmcnt(30)\n\t" \
    "s_barrier\n\t" \
    "buffer_load_dwordx4 v2, s[36:39], s48 offen lds\n\t" \
    "s_add_u32 m0, m0, 0x400\n\t" \
    "s_mul_i32 s49, s53, 3\n\t" \
    "buffer_load_dwordx4 v3, s[36:39], s48 offen lds\n\t" \
    "s_add_u32 m0, m0, 0x3c00\n\t" \
    "s_add_u32 s48, s49, s60\n\t" \
    "buffer_load_dwordx4 v2, s[36:39], s48 offen lds\n\t" \
    "s_add_u32 m0, m0, 0x400\n\t" \
    "s_nop 0\n\t" \
    "buffer_load_dwordx4 v3, s[36:39], s48 offen lds\n\t" \
    "s_add_u32 m0, m0, 0xc00\n\t" \
    "s_add_u32 s48, s48, s54\n\t" \
    "buffer_load_dwordx4 v2, s[36:39], s48 offen lds\n\t" \
    "s_add_u32 m0, m0, 0x400\n\t" \
    "s_nop 0\n\t" \
    "buffer_load_dwordx4 v3, s[36:39], s48 offen lds\n\t" \
    "s_add_u32 m0, m0, 0xc00\n\t" \
    "s_add_u32 s48, s48, s54\n\t" \
    "s_waitcnt vmcnt(32)\n\t" \
    "s_barrier\n\t" \
    "buffer_load_dwordx4 v2, s[36:39], s48 offen lds\n\t" \
    "s_add_u32 m0, m0, 0x400\n\t" \
    "s_nop 0\n\t" \
    "buffer_load_dwordx4 v3, s[36:39], s48 offen lds\n\t" \
    "s_add_u32 m0, m0, 0xc00\n\t" \
    "s_add_u32 s48, s48, s54\n\t" \
    "buffer_load_dwordx4 v2, s[36:39], s48 offen lds\n\t" \
    "s_add_u32 m0, m0, 0x400\n\t" \
    "s_nop 0\n\t" \
    "buffer_load_dwordx4 v3, s[36:39], s48 offen lds\n\t" \
    "s_add_u32 m0, m0, 0xc00\n\t" \
    "s_add_u32 s48, s48, s54\n\t" \
    "s_waitcnt vmcnt(32)\n\t" \
    "s_barrier\n\t" \
    "buffer_load_dwordx4 v2, s[36:39], s48 offen lds\n\t" \
    "s_add_u32 m0, m0, 0x400\n\t" \
    "s_nop 0\n\t" \
    "buffer_load_dwordx4 v3, s[36:39], s48 offen lds\n\t" \
    "s_add_u32 m0, m0, 0xc00\n\t" \
    "s_add_u32 s48, s48, s54\n\t" \
    "buffer_load_dwordx4 v2, s[36:39], s48 offen lds\n\t" \
    "s_add_u32 m0, m0, 0x400\n\t" \
    "s_nop 0\n\t" \
    "buffer_load_dwordx4 v3, s[36:39], s48 offen lds\n\t" \
    "s_waitcnt vmcnt(28)\n\t" \
    "s_barrier\n\t" \
    "s_waitcnt vmcnt(24)\n\t" \
    "s_barrier\n\t" \
    "s_barrier\n\t" \
    "s_waitcnt vmcnt(18)\n\t" \
    "s_barrier\n\t" \
    "s_waitcnt vmcnt(12)\n\t" \
    "s_barrier\n\t" \
    "s_barrier\n\t" \
    "s_waitcnt vmcnt(8)\n\t" \
    "s_barrier\n\t" \
    "s_waitcnt vmcnt(4)\n\t" \
    "s_barrier\n\t" \
    "s_waitcnt vmcnt(0)\n\t" \
    "s_barrier\n\t" \
    "L_fb_%=:\n\t" \
    "s_barrier\n\t" \
    "L_end_%=:\n\t"
#define LOADER_CLOBBERS "s36", "s37", "s38", "s39", "s40", "s41", "s42", "s43", "s44", "s45", "s46", "s47", "s48", "s49", "s50", "s51", "s52", "s53", "s54", "s55", "s56", "s57", "s58", "s59", "s60", "s61", "s62", "s63", "s64", "s65", "s66", "s67", "vcc", "v2", "v3", "v4", "v5", "v6", "v7", "v8", "v9", "v10", "v11", "v12", "v13", "v14", "v15"

__device__ __forceinline__ float bf2f(u16 h) { return __uint_as_float((unsigned)h << 16); }
__device__ __forceinline__ void swap16(float v, float& a, float& b) { auto r = __builtin_amdgcn_permlane16_swap(__float_as_uint(v), __float_as_uint(v), false, false); a = __uint_as_float(r[0]); b = __uint_as_float(r[1]); }
__device__ __forceinline__ void swap32(float v, float& a, float& b) { auto r = __builtin_amdgcn_permlane32_swap(__float_as_uint(v), __float_as_uint(v), false, false); a = __uint_as_float(r[0]); b = __uint_as_float(r[1]); }
__device__ __forceinline__ float xsum8(float v) { return v + __uint_as_float(__builtin_amdgcn_update_dpp(0u, __float_as_uint(v), 0x128, 0xf, 0xf, true)); }

__global__ __launch_bounds__(640, 3) void conv_gn_gelu(const u16* __restrict__ x, const u16* __restrict__ w, const u16* __restrict__ bias,
                                                    const u16* __restrict__ gamma, const u16* __restrict__ beta,
                                                    u16* __restrict__ out, unsigned* ws) {
    extern __shared__ __attribute__((aligned(16))) char lds_[];
    LAS char* lds = (LAS char*)lds_;
    unsigned bid = blockIdx.x, tid = threadIdx.x;
    unsigned long long pc;
    asm volatile(LOADER_ASM
        : [pc] "=&s"(pc), [bid] "+s"(bid), [tid] "+v"(tid)
        : [ka] "s"(__builtin_amdgcn_kernarg_segment_ptr())
        : "memory", "scc", LOADER_CLOBBERS);
    static_assert(WOFF == 36864 && X0OFF == 122880 && XREG == 18432 && TAIL_BARRIERS == 13, "constants in the loader code");

    int lane = tid & 63;
    const int wave0 = __builtin_amdgcn_readfirstlane(tid >> 6);
    const int wave = wave0 < 6 ? wave0 + 4 : wave0 - 6;
    const int kh = bid & 1, ntile = (bid >> 3) & 3, ml = bid >> 5;
    const int bat = (bid >> 2) & 1, mt = 8 * ((bid >> 1) & 1) + ml;
    const int rot = (ml + ntile) & 3;
    const int mir = ((ml >> 1) ^ (ml >> 2)) & 1;
    const int m0 = mt * 128;
    const int grp = bat * 4 + ntile;
    const int col0 = ntile * 64 + kh * 32;
    const int rec0 = WS_REC + (bat * 16 + ntile * 4 + kh * 2) * 4096;
    unsigned a_out, a_y, a_r;
    auto finals = [&]() {
        const int row = wave * 16 + (lane >> 2), cg = lane & 3;
        const int orow = m0 + (mir ? 127 - row : row);
        a_out = ((bat * L_ + orow) * D_ + col0 + cg * 8) * 2; a_y = (unsigned)(size_t)lds + YT_OFF + (row * YT_STRIDE + cg * 8) * 4; a_r = (unsigned)(size_t)lds + RES_OFF + cg * 32;
        asm volatile("" : "+v"(a_out), "+v"(a_y), "+v"(a_r));
    };
    auto finish = [&]() __attribute__((always_inline)) {
    asm volatile("s_waitcnt lgkmcnt(0)\n\ts_barrier" ::: "memory");
    const __amdgpu_buffer_rsrc_t rso = __builtin_amdgcn_make_buffer_rsrc((void*)out, 0, (bat + 1) * (L_ * D_ * 2), 0x00020000);
    {
        const f32x4 sa = *(const LAS f32x4*)(size_t)(a_r), ha = *(const LAS f32x4*)(size_t)(a_r + 256), ya = *(const LAS f32x4*)(size_t)(a_y);
        const f32x4 sb = *(const LAS f32x4*)(size_t)(a_r + 16), hb = *(const LAS f32x4*)(size_t)(a_r + 272), yb = *(const LAS f32x4*)(size_t)(a_y + 16);
        const f32x2 c1 = {-0.1029432397f, -0.1029432397f}, c0 = {-2.302208198f, -2.302208198f};
        f32x2 z[4] = {ya.xy * sa.xy + ha.xy, ya.zw * sa.zw + ha.zw, yb.xy * sb.xy + hb.xy, yb.zw * sb.zw + hb.zw};
        u32x4 o;
#pragma unroll
        for (int e = 0; e < 4; ++e) {
            f32x2 a = z[e] * z[e];
            asm("v_pk_fma_f32 %0, %0, %1, %2" : "+v"(a) : "v"(c1), "v"(c0));
            a *= z[e];
            f32x2 d = (f32x2){__builtin_amdgcn_exp2f(a.x), __builtin_amdgcn_exp2f(a.y)} + 1.0f;
            const f32x2 gl = z[e] * (f32x2){__builtin_amdgcn_rcpf(d.x), __builtin_amdgcn_rcpf(d.y)};
            o[e] = __builtin_bit_cast(unsigned, __builtin_convertvector(gl, bf16x2));
        }
        __builtin_amdgcn_raw_buffer_store_b128(o, rso, a_out, 0, 17);
    }
    if (mt == 0 && kh == 0 && wave == 0 && lane == 0) __hip_atomic_store((GAS unsigned*)ws + grp * 64, *(const LAS unsigned*)(lds + EPO_OFF) + 1, __ATOMIC_RELAXED, __HIP_MEMORY_SCOPE_AGENT);
    };
    if (wave0 >= 2 && wave0 < 6) {
        finals();
        asm volatile("s_barrier" ::: "memory");
        asm volatile("; finish A");
        finish();
    } else if (wave0 < 2) {
        const int cb = wave0, rho = (lane >> 4) & 1, cc = cb * 16 + 2 * (lane & 7) + rho;
        f32x4 prm = {bf2f(*(const LAS u16*)(lds + PRM_OFF + cc * 2)), bf2f(*(const LAS u16*)(lds + PRM_OFF + 256 + cc * 2)), bf2f(*(const LAS u16*)(lds + PRM_OFF + 512 + cc * 2)),
                     (lane >= 56) ? (float)(L_ - 31 * 64) / 64.f : 1.f};
        const __amdgpu_buffer_rsrc_t rsw = __builtin_amdgcn_make_buffer_rsrc((void*)ws, 0, WS_BYTES, 0x00020000);
        unsigned o0 = rec0 + cb * 4096 + lane * 16, op = rec0 + cb * 4096 + (lane & 7) * 16, a_res = (unsigned)(size_t)lds + RES_OFF + cc * 4;
        finals();
        asm volatile("s_barrier\n\ts_sleep 13" : "+v"(prm), "+v"(o0), "+v"(op), "+v"(a_res) :: "memory");
        u32x4 rc[4], rp;
        int sp = 0;
#pragma unroll 1
        for (; sp < SPIN_CAP; ++sp) {
#pragma unroll
            for (int i = 0; i < 4; ++i) rc[i] = __builtin_amdgcn_raw_buffer_load_b128(rsw, o0 + i * 1024, 0, 16);
            rp = __builtin_amdgcn_raw_buffer_load_b128(rsw, op, 0, 16);
            const unsigned bad = (rc[0].y | rc[0].w | rc[1].y) | (rc[1].w | rc[2].y | rc[2].w) | (rc[3].y | rc[3].w | rp.y | rp.w);
            if (__builtin_expect(!__any((int)bad < 0), 1)) break;
            asm volatile("" ::: "memory");
        }
        float A[2], B[2];
#pragma unroll
        for (int c = 0; c < 2; ++c) {
            const float pv = __uint_as_float(rp[2 * c]);
            const float d0 = __uint_as_float(rc[0][2 * c]) - pv, d1 = __uint_as_float(rc[1][2 * c]) - pv, d2 = __uint_as_float(rc[2][2 * c]) - pv, d3 = __uint_as_float(rc[3][2 * c]) - pv;
            const float e3 = prm.w * d3;
            const float a = (d0 + d1) + (d2 + e3);
            const float b = 64.f * (d0 * d0 + d1 * d1 + d2 * d2 + e3 * d3) + ((__uint_as_float(rc[0][2 * c + 1]) + __uint_as_float(rc[1][2 * c + 1])) + (__uint_as_float(rc[2][2 * c + 1]) + __uint_as_float(rc[3][2 * c + 1])));
            A[c] = xsum8(a); B[c] = xsum8(b);
        }
        float As, Bs;
        { auto r = __builtin_amdgcn_permlane16_swap(__float_as_uint(A[0]), __float_as_uint(A[1]), false, false); As = __uint_as_float(r[0]) + __uint_as_float(r[1]); }
        { auto r = __builtin_amdgcn_permlane16_swap(__float_as_uint(B[0]), __float_as_uint(B[1]), false, false); Bs = __uint_as_float(r[0]) + __uint_as_float(r[1]); }
        { float u, w2; swap32(As, u, w2); As = (u + w2) * (64.0f / (float)L_); swap32(Bs, u, w2); Bs = (u + w2) * (1.0f / (float)L_); }
        const float piv = __uint_as_float(rho ? rp.z : rp.x);
        const float var = Bs - As * As;
        const float sc = prm.y * __builtin_amdgcn_rsqf(var + 1e-5f);
        const float mu = (piv + As) + prm.x;
        *(LAS float*)(size_t)(a_res) = sc;
        *(LAS float*)(size_t)(a_res + 256) = prm.z + (prm.x - mu) * sc;
        asm volatile("; finish B");
        finish();
    } else {
        const int nh = wave & 1, mh = wave >> 1;
        const int r = lane & 15, gam = lane >> 4;
        __builtin_amdgcn_s_setprio(1);
        {
            const unsigned long long pp0 = wave == 0 ? (unsigned long long)bias : wave == 1 ? (unsigned long long)gamma : (unsigned long long)beta;
            const unsigned long long pa = wave == 3 ? (unsigned long long)ws + grp * 256 : pp0 + col0 * 2;
            const unsigned long long pc2 = (pc & ~127ull) + min(((bid >> 3) * 4 + wave) * 128, CODE_SPAN);
            const unsigned long long ad = lane < 16 ? pa + lane * 4 : pc2;
            __builtin_amdgcn_global_load_lds((const GAS void*)ad, (LAS void*)(lds + PRM_OFF + wave * 256), 4, 0, 16);
            __builtin_amdgcn_sched_barrier(0);
        }
        unsigned xb[3];
#pragma unroll
        for (int c = 0; c < 3; ++c) xb[c] = X0OFF + mh * 8704 + (4 * r + 4 * c + ((r + c) >> 2)) * 128 + ((4 * (rot & 1) + gam) ^ (((r + c) & 3) << 1)) * 16;
        const int q = r >> 2, pp = r & 3;
        const int f = ((q >> 1) & 1) + 2 * (gam & 1);
        unsigned wb[2];
#pragma unroll
        for (int n = 0; n < 2; ++n) wb[n] = WOFF + (8 * gam + q) * 128 + (((2 * (n ^ kh) + nh) ^ f) * 32) + pp * 8;
        unsigned nb0, nb1;
        const int st = 2 * mt + (mh ^ mir);
        unsigned a_rec = rec0 + nh * 4096 + st * 128 + r * 8 + (lane >= 16 ? 0x40000000u : 0u);
        const __amdgpu_buffer_rsrc_t rsw = __builtin_amdgcn_make_buffer_rsrc((void*)ws, 0, WS_BYTES, 0x00020000);
        asm volatile("" : "+v"(a_rec));
        f32x4 y[4];
        {
            unsigned s_cnt = 1, s_dl = (unsigned)(X1OFF - X0OFF);
            const unsigned sa = WS_X + bid * 8192 + wave * 2048, ra = sa ^ 8192;
            asm volatile(MAIN_LOOP_ASM
                         : "=&{v[24:27]}"(y[0]), "=&{v[28:31]}"(y[1]), "=&{v[32:35]}"(y[2]), "=&{v[36:39]}"(y[3]),
                           [xb0] "+v"(xb[0]), [xb1] "+v"(xb[1]), [xb2] "+v"(xb[2]), [nb0] "=&v"(nb0), [nb1] "=&v"(nb1), [cnt] "+s"(s_cnt), [dl] "+s"(s_dl)
                         : [wb0] "v"(wb[0]), [wb1] "v"(wb[1]), [l16] "v"(lane * 16), [rs] "s"(rsw), [sa] "s"(sa), [ra] "s"(ra)
                         : "memory", "scc", "vcc", MAIN_LOOP_CLOBBERS);
        }
        float mean, m2;
        if (__builtin_expect(st != 31, 1)) {
            const f32x4 s4 = (y[0] + y[1]) + (y[2] + y[3]);
            const f32x2 s2 = s4.xy + s4.zw;
            float m = (s2.x + s2.y) * 0.0625f;
            asm volatile("" : "+v"(m));
            const f32x2 nm = {-m, -m};
            f32x2 qa2, qb2;
#pragma unroll
            for (int t = 0; t < 4; ++t) {
                f32x2 da, db;
                asm("v_pk_add_f32 %0, %1, %2" : "=v"(da) : "v"(y[t].xy), "v"(nm));
                asm("v_pk_add_f32 %0, %1, %2" : "=v"(db) : "v"(y[t].zw), "v"(nm));
                if (t == 0) { qa2 = da * da; qb2 = db * db; } else { qa2 += da * da; qb2 += db * db; }
            }
            const f32x2 q2 = qa2 + qb2;
            float qq = q2.x + q2.y;
            float ma, mb, qa, qb;
            swap16(m, ma, mb); swap16(qq, qa, qb);
            { const float d = ma - mb; qq = (qa + qb) + 8.f * d * d; m = 0.5f * (ma + mb); }
            swap32(m, ma, mb); swap32(qq, qa, qb);
            { const float d = ma - mb; m2 = (qa + qb) + 16.f * d * d; mean = 0.5f * (ma + mb); }
        } else {
            constexpr int NV = L_ - 31 * 64;
            float s = 0.f;
#pragma unroll
            for (int i = 0; i < NV; ++i) s += y[i & 3][i >> 2];
            mean = s * (1.0f / NV);
            m2 = 0.f;
#pragma unroll
            for (int i = 0; i < NV; ++i) { const float d = y[i & 3][i >> 2] - mean; m2 += d * d; }
        }
        {
            u32x2 rec = {__float_as_uint(mean), __float_as_uint(m2) & 0x7fffffffu};
            __builtin_amdgcn_raw_buffer_store_b64(rec, rsw, a_rec, 0, 16);
        }
        asm volatile("s_barrier" ::: "memory");
        asm volatile("" : "+v"(lane));
        const unsigned a_yt = (unsigned)(size_t)lds + YT_OFF + ((64 * mh + 16 * (lane >> 4)) * YT_STRIDE + 16 * nh + (lane & 15)) * 4;
#pragma unroll
        for (int t = 0; t < 4; ++t)
#pragma unroll
            for (int e = 0; e < 4; ++e) *(LAS float*)(size_t)(a_yt + (4 * e + t) * YT_STRIDE * 4) = y[t][e];
        finals();
        asm volatile("; finish C");
        finish();
    }
}

extern "C" void kernel_launch(void* const* d_in, const int* in_sizes, int n_in,
                              void* d_out, int out_size, void* d_ws, size_t ws_size,
                              hipStream_t stream) {
    (void)hipFuncSetAttribute((const void*)conv_gn_gelu, hipFuncAttributeMaxDynamicSharedMemorySize, LDS_BYTES);
    conv_gn_gelu<<<256, 640, LDS_BYTES, stream>>>((const u16*)d_in[0], (const u16*)d_in[1], (const u16*)d_in[2], (const u16*)d_in[3], (const u16*)d_in[4],
                                                  (u16*)d_out, (unsigned*)d_ws);
}
```
